```python
import torch

def _softmax(x, axis=-1):
    return torch.softmax(x, dim=axis)

def _gelu(x):
    return torch.nn.functional.gelu(x, approximate='tanh')

def _silu(x):
    return torch.nn.functional.silu(x)

def _sigmoid(x):
    return torch.sigmoid(x)

def _layernorm(x, axis=-1, eps=1e-05):
    m = x.mean(dim=axis, keepdim=True)
    v = x.var(dim=axis, keepdim=True, unbiased=False)
    return (x - m) / torch.sqrt(v + eps)

def _scatter_add(tgt, idx, src, axis=0):
    return tgt.clone().index_add_(axis, idx, src)

def _segment_sum(src, seg, n):
    out = torch.zeros((n,) + src.shape[1:], dtype=src.dtype)
    return out.index_add_(0, seg, src)

def _pad(x, pw):
    flat = tuple((v for pair in reversed(pw) for v in pair))
    return torch.nn.functional.pad(x, flat)
kernel_name = 'adaLN_cond__gqa_block_cached__glu_gated_bilinear__decode'

def setup_inputs():

    _w = lambda a: (lambda t: t.to(torch.bfloat16) if t.is_floating_point() else t)(torch.as_tensor(a))

    def _A_setup():
        B, Sq, D = (2, 1, 512)
        Hq, Hkv, Sk, Dh = (32, 8, 32768, 128)
        assert Hq % Hkv == 0, (Hq, Hkv)
        return {'x': _w((lambda *s: torch.randn(*s, dtype=torch.bfloat16) * 0.1)(B, Sq, D)), 'k_cache': _w((lambda *s: torch.randn(*s, dtype=torch.bfloat16) * 0.8)(B, Hkv, Sk, Dh)), 'v_cache': _w((lambda *s: torch.randn(*s, dtype=torch.bfloat16) * 12.8)(B, Hkv, Sk, Dh)), 'wq': _w((lambda *s: torch.randn(*s, dtype=torch.bfloat16) * 0.8)(D, Hq * Dh) * D ** (-0.5)), 'wo': _w((lambda *s: torch.randn(*s, dtype=torch.bfloat16) * 12.8)(Hq * Dh, D) * (Hq * Dh) ** (-0.5))}
    _A_in = _A_setup()

    def _B_setup():
        B, S, D, F = (2, 1, 512, 1792)
        return {'x': _w((lambda *s: torch.randn(*s, dtype=torch.bfloat16) * 0.1)(B, S, D)), 'w_gate': _w((lambda *s: torch.randn(*s, dtype=torch.bfloat16) * 12.8)(D, F) * D ** (-0.5)), 'w_up': _w((lambda *s: torch.randn(*s, dtype=torch.bfloat16) * 12.8)(D, F) * D ** (-0.5)), 'w_down': _w((lambda *s: torch.randn(*s, dtype=torch.bfloat16) * 12.8)(F, D) * F ** (-0.5))}
    _B_in = _B_setup()
    return {'A_x': _A_in['x'], 'A_k_cache': _A_in['k_cache'], 'A_v_cache': _A_in['v_cache'], 'A_wq': _A_in['wq'], 'A_wo': _A_in['wo'], 'B_x': _B_in['x'], 'B_w_gate': _B_in['w_gate'], 'B_w_up': _B_in['w_up'], 'B_w_down': _B_in['w_down'], 'c': _w((lambda *s: torch.randn(*s, dtype=torch.bfloat16) * 0.2)(2, 512) * 8.0), 'w_mod': _w((lambda *s: torch.randn(*s, dtype=torch.bfloat16) * 0.2)(512, 6 * 512) * 512 ** (-0.5) * 8.0)}

def reference(A_x, A_k_cache, A_v_cache, A_wq, A_wo, B_x, B_w_gate, B_w_up, B_w_down, c, w_mod):

    def _A_ref(x, k_cache, v_cache, wq, wo):
        B, Sq, D = x.shape
        Hq, Dh = (32, 128)
        Hkv = k_cache.shape[1]
        rep = Hq // Hkv
        q = (lambda x, *d: x.permute(*d))((x @ wq).reshape(B, Sq, Hq, Dh), 0, 2, 1, 3)
        k = (lambda x, n, axis: torch.repeat_interleave(x, n, dim=axis))(k_cache, rep, axis=1)
        v = (lambda x, n, axis: torch.repeat_interleave(x, n, dim=axis))(v_cache, rep, axis=1)
        scores = torch.einsum('bhqd,bhkd->bhqk', q, k) * Dh ** (-0.5)
        p = _softmax(scores, axis=-1)
        o = torch.einsum('bhqk,bhkd->bhqd', p, v)
        return (lambda x, *d: x.permute(*d))(o, 0, 2, 1, 3).reshape(B, Sq, Hq * Dh) @ wo

    def _B_ref(x, w_gate, w_up, w_down):
        g = torch.einsum('bsd,df->bsf', x, w_gate)
        u = torch.einsum('bsd,df->bsf', x, w_up)
        h = g * u
        return torch.einsum('bsf,fd->bsd', h, w_down)
    x = A_x
    mod = (_silu(c) @ w_mod).reshape(c.shape[0], 6, 1, -1)
    sa, ca, ga, sb, cb, gb = (mod[:, 0].clone(), mod[:, 1].clone(), mod[:, 2].clone(), mod[:, 3].clone(), mod[:, 4].clone(), mod[:, 5].clone())
    h = _layernorm(x, axis=-1) * (1 + ca) + sa
    x = x + ga * _A_ref(h, A_k_cache, A_v_cache, A_wq, A_wo)
    h = _layernorm(x, axis=-1) * (1 + cb) + sb
    x = x + gb * _B_ref(h, B_w_gate, B_w_up, B_w_down)
    return x
```

```cpp
#include <hip/hip_runtime.h>
#include <hip/hip_bf16.h>
#include <cstdint>
#include <cstdio>

using bf16 = __hip_bfloat16;
using u32x4  = __attribute__((ext_vector_type(4))) unsigned;
using f32x4  = __attribute__((ext_vector_type(4))) float;
using bf16x2 = __attribute__((ext_vector_type(2))) __bf16;

constexpr int D_MODEL = 512, HQ = 32, HKV = 8, SK = 32768, DH = 128, FF = 1792, NB = 2;
constexpr int NSPLIT = 32;
constexpr int PAGE = 16, WAVES = 4, NTHR = 256;
constexpr int PPS = SK / PAGE / NSPLIT, PPW = PPS / WAVES;
constexpr float SCALE = 0.088388347648318440f;

constexpr size_t WS_MOD = 0;
constexpr size_t WS_Q   = 32768;
constexpr size_t WS_PML = 65536;
constexpr size_t WS_PO  = WS_PML + (size_t)NB * HQ * NSPLIT * 2 * 4;
constexpr size_t WS_YP  = WS_PO + (size_t)NB * HQ * NSPLIT * DH * 4;
constexpr size_t WS_HH  = WS_YP + 16 * 2 * 512 * 4;
constexpr size_t WS_X2  = WS_HH + 2 * 1792 * 4;
constexpr size_t WS_OP  = WS_X2 + 2 * 512 * 4;

__device__ __forceinline__ float bf2f(unsigned short u) { return __builtin_bit_cast(float, (unsigned)u << 16); }
__device__ __forceinline__ float lo2f(unsigned u) { return __builtin_bit_cast(float, u << 16); }
__device__ __forceinline__ float hi2f(unsigned u) { return __builtin_bit_cast(float, u & 0xffff0000u); }
__device__ __forceinline__ unsigned cvtpk(float lo, float hi) {
  unsigned r; asm volatile("v_cvt_pk_bf16_f32 %0, %1, %2" : "=v"(r) : "v"(lo), "v"(hi)); return r;
}
__device__ __forceinline__ float rbf(float x) { return lo2f(cvtpk(x, x)); }
__device__ __forceinline__ float dot2(unsigned k, unsigned q, float acc) {
  return __builtin_amdgcn_fdot2_f32_bf16(__builtin_bit_cast(bf16x2, k), __builtin_bit_cast(bf16x2, q), acc, false);
}
template <int CTRL> __device__ __forceinline__ float dpp(float x) {
  return __builtin_bit_cast(float, __builtin_amdgcn_mov_dpp(__builtin_bit_cast(int, x), CTRL, 0xf, 0xf, true));
}
constexpr int XOR1 = 0xB1, XOR2 = 0x4E, XOR7 = 0x141, XOR8 = 0x128, ROR4 = 0x124;
__device__ __forceinline__ float xrow16_max(float x) {
  auto s = __builtin_amdgcn_permlane16_swap(__float_as_uint(x), __float_as_uint(x), false, false);
  x = fmaxf(__uint_as_float(s[0]), __uint_as_float(s[1]));
  auto t = __builtin_amdgcn_permlane32_swap(__float_as_uint(x), __float_as_uint(x), false, false);
  return fmaxf(__uint_as_float(t[0]), __uint_as_float(t[1]));
}
__device__ __forceinline__ float xrow16_sum(float x) {
  auto s = __builtin_amdgcn_permlane16_swap(__float_as_uint(x), __float_as_uint(x), false, false);
  x = __uint_as_float(s[0]) + __uint_as_float(s[1]);
  auto t = __builtin_amdgcn_permlane32_swap(__float_as_uint(x), __float_as_uint(x), false, false);
  return __uint_as_float(t[0]) + __uint_as_float(t[1]);
}

template <int K, int NCH>
__device__ __forceinline__ void gemv2(const float* a, const bf16* W, int ldw, int col0, float* red) {
  constexpr int RG = 256 / NCH, RPT = K / RG;
  const int tid = threadIdx.x, ch = tid % NCH, rg = tid / NCH;
  u32x4 w[RPT];
#pragma unroll
  for (int i = 0; i < RPT; ++i) w[i] = __builtin_nontemporal_load(reinterpret_cast<const u32x4*>(W + (size_t)(rg + i * RG) * ldw + col0 + ch * 8));
  float acc[2][8];
#pragma unroll
  for (int j = 0; j < 8; ++j) acc[0][j] = acc[1][j] = 0.f;
#pragma unroll
  for (int i = 0; i < RPT; ++i) {
    const float a0 = a[rg + i * RG], a1 = a[K + rg + i * RG];
#pragma unroll
    for (int j = 0; j < 4; ++j) {
      const float l = lo2f(w[i][j]), h = hi2f(w[i][j]);
      acc[0][2 * j] = fmaf(a0, l, acc[0][2 * j]); acc[0][2 * j + 1] = fmaf(a0, h, acc[0][2 * j + 1]);
      acc[1][2 * j] = fmaf(a1, l, acc[1][2 * j]); acc[1][2 * j + 1] = fmaf(a1, h, acc[1][2 * j + 1]);
    }
  }
#pragma unroll
  for (int off = NCH; off < 64; off <<= 1)
#pragma unroll
    for (int j = 0; j < 8; ++j) { acc[0][j] += __shfl_xor(acc[0][j], off); acc[1][j] += __shfl_xor(acc[1][j], off); }
  __shared__ float part[4][2][8 * NCH];
  const int lane = tid & 63, wave = tid >> 6;
  if (lane < NCH) {
#pragma unroll
    for (int j = 0; j < 8; ++j) { part[wave][0][lane * 8 + j] = acc[0][j]; part[wave][1][lane * 8 + j] = acc[1][j]; }
  }
  __syncthreads();
  if (tid < 16 * NCH) { const int b = tid / (8 * NCH), c = tid % (8 * NCH); red[tid] = part[0][b][c] + part[1][b][c] + part[2][b][c] + part[3][b][c]; }
  __syncthreads();
}

__global__ void __launch_bounds__(256) k_mod(const bf16* c, const bf16* wmod, float* mod) {
  __shared__ float a[2 * 512]; __shared__ float red[64];
  const int tid = threadIdx.x;
  for (int i = tid; i < 1024; i += 256) { const float x = __bfloat162float(c[i]); a[i] = rbf(x / (1.f + __expf(-x))); }
  __syncthreads();
  gemv2<512, 4>(a, wmod, 3072, blockIdx.x * 32, red);
  if (tid < 64) { const int b = tid / 32, cc = tid % 32; mod[b * 3072 + blockIdx.x * 32 + cc] = rbf(red[tid]); }
}

__device__ __forceinline__ void ln_mod(const float* xs, const float* mod, int soff, int coff, float* a) {
  const int tid = threadIdx.x, lane = tid & 63, wave = tid >> 6;
  if (wave < 2) {
    const float* x = xs + wave * 512;
    float s = 0.f;
    for (int i = lane; i < 512; i += 64) s += x[i];
#pragma unroll
    for (int o = 32; o; o >>= 1) s += __shfl_xor(s, o);
    const float m = rbf(s * (1.f / 512.f));
    float v = 0.f;
    for (int i = lane; i < 512; i += 64) { const float d = x[i] - s * (1.f / 512.f); v += d * d; }
#pragma unroll
    for (int o = 32; o; o >>= 1) v += __shfl_xor(v, o);
    v = rbf(v * (1.f / 512.f));
    const float den = rbf(sqrtf(rbf(v + 1e-5f)));
    for (int i = lane; i < 512; i += 64) {
      const float n = rbf(rbf(x[i] - m) / den);
      const float cm = mod[wave * 3072 + coff + i], sm = mod[wave * 3072 + soff + i];
      a[wave * 512 + i] = rbf(rbf(n * rbf(1.f + cm)) + sm);
    }
  }
  __syncthreads();
}

__global__ void __launch_bounds__(256) k_q(const bf16* x, const float* mod, const bf16* wq, unsigned short* q) {
  __shared__ float xs[1024]; __shared__ float a[1024]; __shared__ float red[32];
  const int tid = threadIdx.x;
  for (int i = tid; i < 1024; i += 256) xs[i] = __bfloat162float(x[i]);
  __syncthreads();
  ln_mod(xs, mod, 0, 512, a);
  gemv2<512, 2>(a, wq, 4096, blockIdx.x * 16, red);
  if (tid < 32) { const int b = tid / 16, cc = tid % 16; q[b * 4096 + blockIdx.x * 16 + cc] = (unsigned short)cvtpk(red[tid], 0.f); }
}

constexpr int LDS_P = 0, LDS_MRG = WAVES * 64, MRG_REC = 4 + DH, LDS_FLOATS = LDS_MRG + WAVES * 4 * MRG_REC;
__global__ void __launch_bounds__(NTHR) k_attn(const unsigned short* qg, const bf16* kc, const bf16* vc, float* pml, float* po) {
  __shared__ __attribute__((aligned(16))) float lds[LDS_FLOATS];
  const int s = blockIdx.x, kvh = blockIdx.y, b = blockIdx.z;
  const int tid = threadIdx.x, lane = tid & 63, wave = __builtin_amdgcn_readfirstlane(tid >> 6), r = lane >> 4, c = lane & 15;
  constexpr float C2 = SCALE * 1.4426950408889634f;
  u32x4 qpk[4];
#pragma unroll
  for (int g = 0; g < 4; ++g) qpk[g] = *reinterpret_cast<const u32x4*>(qg + ((size_t)b * HQ + kvh * 4 + g) * DH + 8 * c);
  const size_t base = ((size_t)(b * HKV + kvh) * SK + (size_t)s * PPS * PAGE) * DH + (size_t)r * DH + 8 * c;
#define ISSUE(i_) do { const bf16* kb_ = kc + base + (size_t)(wave + 4 * (i_)) * PAGE * DH; const bf16* vb_ = vc + base + (size_t)(wave + 4 * (i_)) * PAGE * DH; \
    _Pragma("unroll") for (int t_ = 0; t_ < 4; ++t_) { Kn[t_] = __builtin_nontemporal_load(reinterpret_cast<const u32x4*>(kb_ + (size_t)t_ * 4 * DH)); Vn[t_] = __builtin_nontemporal_load(reinterpret_cast<const u32x4*>(vb_ + (size_t)t_ * 4 * DH)); } } while (0)
  u32x4 Kn[4], Vn[4];
  float m = -INFINITY, l = 0.f, acc[32];
#pragma unroll
  for (int k = 0; k < 32; ++k) acc[k] = 0.f;
  ISSUE(0);
  for (int i = 0; i < PPW; ++i) {
    u32x4 Kc[4], Vc[4];
#pragma unroll
    for (int t = 0; t < 4; ++t) { Kc[t] = Kn[t]; Vc[t] = Vn[t]; }
    if (i + 1 < PPW) ISSUE(i + 1);
    float x[16];
#pragma unroll
    for (int t = 0; t < 4; ++t)
#pragma unroll
      for (int g = 0; g < 4; ++g) { float d = 0.f;
#pragma unroll
        for (int k = 0; k < 4; ++k) d = dot2(Kc[t][k], qpk[g][k], d);
        x[t * 4 + g] = d; }
#define TR_STEP(HALF, CTRL, BIT) _Pragma("unroll") for (int h = 0; h < (HALF); ++h) { \
      const float lo_ = x[h], hi_ = x[h + (HALF)]; const float t1_ = lo_ + dpp<CTRL>(lo_), t2_ = hi_ + dpp<CTRL>(hi_); \
      x[h] = (c & (BIT)) ? t2_ : t1_; }
    TR_STEP(8, XOR8, 8) TR_STEP(4, XOR7, 4) TR_STEP(2, XOR2, 2) TR_STEP(1, XOR1, 1)
#undef TR_STEP
    const float sc = x[0] * C2;
    float pm = sc; pm = fmaxf(pm, dpp<ROR4>(pm)); pm = fmaxf(pm, dpp<XOR8>(pm)); pm = xrow16_max(pm);
    const float mn = fmaxf(m, pm);
    const float alpha = __builtin_amdgcn_exp2f(m - mn), p = __builtin_amdgcn_exp2f(sc - mn);
    l = fmaf(l, alpha, p); m = mn;
#pragma unroll
    for (int q4 = 0; q4 < 4; ++q4) {
      const float al = q4 == 0 ? dpp<0x00>(alpha) : q4 == 1 ? dpp<0x55>(alpha) : q4 == 2 ? dpp<0xAA>(alpha) : dpp<0xFF>(alpha);
#pragma unroll
      for (int k = 0; k < 8; ++k) acc[q4 * 8 + k] *= al; }
    float* prow = lds + LDS_P + (wave * 4 + r) * 16;
    prow[c] = p;
    asm volatile("s_waitcnt lgkmcnt(0)" ::: "memory");
    f32x4 pr[4];
#pragma unroll
    for (int u = 0; u < 4; ++u) pr[u] = *reinterpret_cast<const f32x4*>(prow + 4 * u);
#pragma unroll
    for (int t = 0; t < 4; ++t) {
      float vf[8];
#pragma unroll
      for (int k = 0; k < 4; ++k) { vf[2 * k] = lo2f(Vc[t][k]); vf[2 * k + 1] = hi2f(Vc[t][k]); }
#pragma unroll
      for (int g = 0; g < 4; ++g) { const float pg_ = pr[t][g];
#pragma unroll
        for (int k = 0; k < 8; ++k) acc[g * 8 + k] = fmaf(pg_, vf[k], acc[g * 8 + k]); } }
  }
#undef ISSUE
#pragma unroll
  for (int k = 0; k < 32; ++k) acc[k] = xrow16_sum(acc[k]);
  { float t = l; t += dpp<ROR4>(t); t += dpp<XOR8>(t); l = xrow16_sum(t); }
  float* mrg = lds + LDS_MRG;
  if (r == 0) {
#pragma unroll
    for (int g = 0; g < 4; ++g) {
      float* rec = mrg + (wave * 4 + g) * MRG_REC;
#pragma unroll
      for (int k = 0; k < 8; k += 4) *reinterpret_cast<f32x4*>(rec + 4 + 8 * c + k) = f32x4{acc[g * 8 + k], acc[g * 8 + k + 1], acc[g * 8 + k + 2], acc[g * 8 + k + 3]};
      if (c == g) { rec[0] = m; rec[1] = l; } } }
  __syncthreads();
  {
    const int g = wave, d0 = 2 * lane;
    const size_t row = (size_t)b * HQ + kvh * 4 + g;
    float M = -INFINITY;
#pragma unroll
    for (int w = 0; w < WAVES; ++w) M = fmaxf(M, mrg[(w * 4 + g) * MRG_REC]);
    float L = 0.f, o0 = 0.f, o1 = 0.f;
#pragma unroll
    for (int w = 0; w < WAVES; ++w) { const float* rec = mrg + (w * 4 + g) * MRG_REC;
      const float wt = __builtin_amdgcn_exp2f(rec[0] - M);
      L = fmaf(wt, rec[1], L); o0 = fmaf(wt, rec[4 + d0], o0); o1 = fmaf(wt, rec[4 + d0 + 1], o1); }
    const size_t slot = row * NSPLIT + s;
    *reinterpret_cast<float2*>(po + slot * DH + d0) = make_float2(o0, o1);
    if (lane == 0) *reinterpret_cast<float2*>(pml + 2 * slot) = make_float2(M, L);
  }
}

__global__ void __launch_bounds__(256) k_wo(const float* pml, const float* po, const bf16* wo, float* yp) {
  __shared__ float a[2 * 256]; __shared__ float red[64];
  const int kg = blockIdx.x / 16, cg = blockIdx.x % 16, tid = threadIdx.x;
  for (int idx = tid; idx < 512; idx += 256) {
    const int b = idx / 256, hd = idx % 256, h = kg * 2 + hd / 128, d = hd % 128;
    const size_t row = (size_t)b * HQ + h;
    float M = -INFINITY;
    for (int s = 0; s < NSPLIT; ++s) M = fmaxf(M, pml[2 * (row * NSPLIT + s)]);
    float L = 0.f, o = 0.f;
    for (int s = 0; s < NSPLIT; ++s) { const float w = __builtin_amdgcn_exp2f(pml[2 * (row * NSPLIT + s)] - M);
      L = fmaf(w, pml[2 * (row * NSPLIT + s) + 1], L); o = fmaf(w, po[(row * NSPLIT + s) * DH + d], o); }
    a[idx] = rbf(o / L);
  }
  __syncthreads();
  gemv2<256, 4>(a, wo + (size_t)kg * 256 * 512, 512, cg * 32, red);
  if (tid < 64) { const int b = tid / 32, cc = tid % 32; yp[(kg * 2 + b) * 512 + cg * 32 + cc] = red[tid]; }
}

__global__ void __launch_bounds__(256) k_gu(const bf16* x, const float* mod, const float* yp, const bf16* wg, const bf16* wu, float* hh, float* x2g) {
  __shared__ float xs[1024]; __shared__ float a[1024]; __shared__ float red[16]; __shared__ float red2[16];
  const int tid = threadIdx.x;
  for (int i = tid; i < 1024; i += 256) {
    float y = 0.f;
#pragma unroll
    for (int k = 0; k < 16; ++k) y += yp[k * 1024 + i];
    const int b = i / 512, d = i % 512;
    const float x2 = rbf(__bfloat162float(x[i]) + rbf(mod[b * 3072 + 1024 + d] * rbf(y)));
    xs[i] = x2;
    if (blockIdx.x == 0) x2g[i] = x2;
  }
  __syncthreads();
  ln_mod(xs, mod, 1536, 2048, a);
  gemv2<512, 1>(a, wg, FF, blockIdx.x * 8, red);
  gemv2<512, 1>(a, wu, FF, blockIdx.x * 8, red2);
  if (tid < 16) { const int b = tid / 8, cc = tid % 8; hh[b * FF + blockIdx.x * 8 + cc] = rbf(rbf(red[tid]) * rbf(red2[tid])); }
}

__global__ void __launch_bounds__(256) k_down(const float* hh, const bf16* wd, float* op) {
  __shared__ float a[2 * 128]; __shared__ float red[64];
  const int kg = blockIdx.x / 16, cg = blockIdx.x % 16, tid = threadIdx.x;
  { const int b = tid / 128, k = tid % 128; a[tid] = hh[b * FF + kg * 128 + k]; }
  __syncthreads();
  gemv2<128, 4>(a, wd + (size_t)kg * 128 * 512, 512, cg * 32, red);
  if (tid < 64) { const int b = tid / 32, cc = tid % 32; op[(kg * 2 + b) * 512 + cg * 32 + cc] = red[tid]; }
}
__global__ void __launch_bounds__(256) k_fin(const float* op, const float* x2g, const float* mod, unsigned short* out) {
  const int i = blockIdx.x * 256 + threadIdx.x;
  float y = 0.f;
#pragma unroll
  for (int k = 0; k < 14; ++k) y += op[k * 1024 + i];
  const int b = i / 512, d = i % 512;
  out[i] = (unsigned short)cvtpk(x2g[i] + rbf(mod[b * 3072 + 2560 + d] * rbf(y)), 0.f);
}

extern "C" void kernel_launch(void* const* d_in, const int* in_sizes, int n_in,
                              void* d_out, int out_size, void* d_ws, size_t ws_size,
                              hipStream_t stream) {
  const bf16* x = (const bf16*)d_in[0]; const bf16* kc = (const bf16*)d_in[1]; const bf16* vc = (const bf16*)d_in[2];
  const bf16* wq = (const bf16*)d_in[3]; const bf16* wo = (const bf16*)d_in[4];
  const bf16* wg = (const bf16*)d_in[6]; const bf16* wu = (const bf16*)d_in[7]; const bf16* wd = (const bf16*)d_in[8];
  const bf16* c = (const bf16*)d_in[9]; const bf16* wmod = (const bf16*)d_in[10];
  char* ws = (char*)d_ws;
  float* mod = (float*)(ws + WS_MOD); unsigned short* q = (unsigned short*)(ws + WS_Q);
  float* pml = (float*)(ws + WS_PML); float* po = (float*)(ws + WS_PO); float* yp = (float*)(ws + WS_YP);
  float* hh = (float*)(ws + WS_HH); float* x2g = (float*)(ws + WS_X2); float* op = (float*)(ws + WS_OP);
  k_mod<<<96, 256, 0, stream>>>(c, wmod, mod);
  k_q<<<256, 256, 0, stream>>>(x, mod, wq, q);
  k_attn<<<dim3(NSPLIT, HKV, NB), 256, 0, stream>>>(q, kc, vc, pml, po);
  k_wo<<<256, 256, 0, stream>>>(pml, po, wo, yp);
  k_gu<<<224, 256, 0, stream>>>(x, mod, yp, wg, wu, hh, x2g);
  k_down<<<224, 256, 0, stream>>>(hh, wd, op);
  k_fin<<<4, 256, 0, stream>>>(op, x2g, mod, (unsigned short*)d_out);
}
```

```cpp
#include <hip/hip_runtime.h>
#include <hip/hip_bf16.h>
#include <cstdint>

using bf16 = __hip_bfloat16;
using u64 = unsigned long long;
using u32x4  = __attribute__((ext_vector_type(4))) unsigned;
using f32x4  = __attribute__((ext_vector_type(4))) float;
using bf16x2 = __attribute__((ext_vector_type(2))) __bf16;

constexpr int HQ = 32, HKV = 8, SK = 32768, DH = 128, FF = 1792;
constexpr int NSPLIT = 16, NWG = 256, NT = 512, NW = 8;
constexpr int PAGE = 16, PPW = SK / NSPLIT / PAGE / NW;
constexpr float SCALE = 0.088388347648318440f;
constexpr unsigned SPIN_CAP = 1u << 22;

constexpr size_t WS_EPOCH = 0;
constexpr size_t WS_GMOD = 4096;
constexpr size_t WS_GQ   = WS_GMOD + 2 * 1536 * 8;
constexpr size_t WS_GML  = WS_GQ + 2 * 2048 * 8;
constexpr size_t WS_PO   = WS_GML + 256 * 8 * 8;
constexpr size_t WS_YP   = WS_PO + 256 * 512 * 4;
constexpr size_t WS_YF   = WS_YP + 16 * 1024 * 4;
constexpr size_t WS_GHH  = WS_YF + 1024;
constexpr size_t WS_GOP  = WS_GHH + 2 * 896 * 8;

struct Params {
  const bf16 *x, *kc, *vc, *wq, *wo, *wg, *wu, *wd, *c, *wmod;
  unsigned short* out; char* ws;
};

__device__ __forceinline__ float lo2f(unsigned u) { return __builtin_bit_cast(float, u << 16); }
__device__ __forceinline__ float hi2f(unsigned u) { return __builtin_bit_cast(float, u & 0xffff0000u); }
__device__ __forceinline__ unsigned cvtpk(float lo, float hi) {
  unsigned r; asm volatile("v_cvt_pk_bf16_f32 %0, %1, %2" : "=v"(r) : "v"(lo), "v"(hi)); return r;
}
__device__ __forceinline__ float rbf(float x) { return lo2f(cvtpk(x, x)); }
__device__ __forceinline__ float dot2(unsigned k, unsigned q, float acc) {
  return __builtin_amdgcn_fdot2_f32_bf16(__builtin_bit_cast(bf16x2, k), __builtin_bit_cast(bf16x2, q), acc, false);
}
template <int CTRL> __device__ __forceinline__ float dpp(float x) {
  return __builtin_bit_cast(float, __builtin_amdgcn_mov_dpp(__builtin_bit_cast(int, x), CTRL, 0xf, 0xf, true));
}
constexpr int XOR1 = 0xB1, XOR2 = 0x4E, XOR7 = 0x141, XOR8 = 0x128, ROR4 = 0x124;
__device__ __forceinline__ float xrow16_max(float x) {
  auto s = __builtin_amdgcn_permlane16_swap(__float_as_uint(x), __float_as_uint(x), false, false);
  x = fmaxf(__uint_as_float(s[0]), __uint_as_float(s[1]));
  auto t = __builtin_amdgcn_permlane32_swap(__float_as_uint(x), __float_as_uint(x), false, false);
  return fmaxf(__uint_as_float(t[0]), __uint_as_float(t[1]));
}
__device__ __forceinline__ float xrow16_sum(float x) {
  auto s = __builtin_amdgcn_permlane16_swap(__float_as_uint(x), __float_as_uint(x), false, false);
  x = __uint_as_float(s[0]) + __uint_as_float(s[1]);
  auto t = __builtin_amdgcn_permlane32_swap(__float_as_uint(x), __float_as_uint(x), false, false);
  return __uint_as_float(t[0]) + __uint_as_float(t[1]);
}
__device__ __forceinline__ float wave_sum(float x) {
  x += dpp<XOR1>(x); x += dpp<XOR2>(x); x += dpp<XOR7>(x); x += dpp<XOR8>(x); return xrow16_sum(x);
}
__device__ __forceinline__ void bar() { asm volatile("s_waitcnt lgkmcnt(0)\n\ts_barrier" ::: "memory"); }
__device__ __forceinline__ void drain() { asm volatile("s_waitcnt vmcnt(0)" ::: "memory"); }
__device__ __forceinline__ u64 ldg(const u64* p) { return __hip_atomic_load(p, __ATOMIC_RELAXED, __HIP_MEMORY_SCOPE_AGENT); }
__device__ __forceinline__ void stg(u64* p, unsigned tag, unsigned v) { __hip_atomic_store(p, ((u64)tag << 32) | v, __ATOMIC_RELAXED, __HIP_MEMORY_SCOPE_AGENT); }
__device__ __forceinline__ unsigned ldw(const unsigned* p) { return __hip_atomic_load(p, __ATOMIC_RELAXED, __HIP_MEMORY_SCOPE_AGENT); }
__device__ __forceinline__ void stw(unsigned* p, unsigned v) { __hip_atomic_store(p, v, __ATOMIC_RELAXED, __HIP_MEMORY_SCOPE_AGENT); }
__device__ __forceinline__ float ldf(const float* p) { return __builtin_bit_cast(float, ldw(reinterpret_cast<const unsigned*>(p))); }
__device__ __forceinline__ void stf(float* p, float v) { stw(reinterpret_cast<unsigned*>(p), __builtin_bit_cast(unsigned, v)); }
__device__ __forceinline__ unsigned wait_g(const u64* p, unsigned tag) {
  u64 x = ldg(p);
  for (unsigned sp = 0; (unsigned)(x >> 32) != tag && sp < SPIN_CAP; ++sp) { __builtin_amdgcn_s_sleep(1); x = ldg(p); }
  return (unsigned)x;
}
template <int N> __device__ __forceinline__ void sweep(const u64* g, int stride, unsigned tag, unsigned (&v)[N]) {
  for (unsigned sp = 0; sp < SPIN_CAP; ++sp) {
    u64 x[N];
#pragma unroll
    for (int k = 0; k < N; ++k) x[k] = ldg(g + (size_t)k * stride);
    bool ok = true;
#pragma unroll
    for (int k = 0; k < N; ++k) { ok = ok && ((unsigned)(x[k] >> 32) == tag); v[k] = (unsigned)x[k]; }
    if (ok) break;
    __builtin_amdgcn_s_sleep(1);
  }
}
__device__ __forceinline__ void wait_w(const unsigned* p, unsigned tag) {
  unsigned x = ldw(p);
  for (unsigned sp = 0; x != tag && sp < SPIN_CAP; ++sp) { __builtin_amdgcn_s_sleep(1); x = ldw(p); }
}
__device__ __forceinline__ void fma8(float (&acc)[16], u32x4 w, float a0, float a1) {
#pragma unroll
  for (int j = 0; j < 4; ++j) {
    const float l = lo2f(w[j]), h = hi2f(w[j]);
    acc[2 * j] = fmaf(a0, l, acc[2 * j]); acc[2 * j + 1] = fmaf(a0, h, acc[2 * j + 1]);
    acc[8 + 2 * j] = fmaf(a1, l, acc[8 + 2 * j]); acc[8 + 2 * j + 1] = fmaf(a1, h, acc[8 + 2 * j + 1]);
  }
}

constexpr int L_PG = 0;
constexpr int L_A = 32768;
constexpr int L_XS = L_A + 1024;
constexpr int L_MOD = L_XS + 1024;
constexpr int L_P = L_MOD + 3072;
constexpr int L_Q = L_P + 512;
constexpr int L_RED = L_Q + 256;
constexpr int L_CW = L_RED + 512;
constexpr int L_END = L_CW + 64;
#define GLDS(src, dst) __builtin_amdgcn_global_load_lds((const unsigned*)(src), (unsigned*)(dst), 16, 0, 2)
__device__ __forceinline__ float modv(const float* lds, int b, int col) {
  const unsigned v = reinterpret_cast<const unsigned*>(lds)[L_MOD + b * 1536 + (col >> 1)]; return (col & 1) ? hi2f(v) : lo2f(v); }

template <int NCH>
__device__ __forceinline__ void reduce_tile(float (&acc)[16], float* lds, int tid, float* res  ) {
  const int lane = tid & 63, wave = tid >> 6;
#pragma unroll
  for (int j = 0; j < 16; ++j) {
    float v = acc[j];
    if (NCH == 1) { v += dpp<XOR1>(v); v += dpp<XOR2>(v); }
    if (NCH == 2) { v += dpp<XOR2>(v); }
    v += dpp<XOR8>(v); v += dpp<ROR4>(v);
    acc[j] = xrow16_sum(v);
  }
  float* part = lds + L_RED;
  if (lane < NCH) {
#pragma unroll
    for (int j = 0; j < 16; ++j) part[(wave * 4 + lane) * 16 + j] = acc[j];
  }
  bar();
  if (tid < NCH * 16) { float s = 0.f;
#pragma unroll
    for (int w = 0; w < 8; ++w) s += part[w * 64 + tid];
    res[tid] = s; }
  bar();
}

__device__ __forceinline__ void ln_mod(float* lds, int soff, int coff, int tid) {
  const int lane = tid & 63, wave = tid >> 6;
  if (wave < 2) {
    const float* x = lds + L_XS + wave * 512;
    float xv[8]; float s = 0.f;
#pragma unroll
    for (int i = 0; i < 8; ++i) { xv[i] = x[lane + 64 * i]; s += xv[i]; }
    s = wave_sum(s) * (1.f / 512.f);
    const float m = rbf(s);
    float v = 0.f;
#pragma unroll
    for (int i = 0; i < 8; ++i) { const float d = xv[i] - s; v += d * d; }
    v = rbf(wave_sum(v) * (1.f / 512.f));
    const float den = rbf(sqrtf(rbf(v + 1e-5f)));
#pragma unroll
    for (int i = 0; i < 8; ++i) {
      const float n = rbf(rbf(xv[i] - m) / den);
      lds[L_A + wave * 512 + lane + 64 * i] = rbf(rbf(n * rbf(1.f + modv(lds, wave, coff + lane + 64 * i))) + modv(lds, wave, soff + lane + 64 * i));
    }
  }
  bar();
}

__global__ void __launch_bounds__(NT, 2) mega(Params p) {
  __shared__ __attribute__((aligned(16))) float lds[L_END];
  const int wg = blockIdx.x, tid = threadIdx.x, lane = tid & 63, wave = __builtin_amdgcn_readfirstlane(tid >> 6);
  const int sw = (wg & 7) * 32 + (wg >> 3);
  const int r = lane >> 4, c = lane & 15;
  const int pair = wg >> 4, s = wg & 15, b = pair >> 3, kvh = pair & 7;
  char* ws = p.ws;
  u64* gmod = (u64*)(ws + WS_GMOD); u64* gq = (u64*)(ws + WS_GQ); u64* gml = (u64*)(ws + WS_GML);
  float* po = (float*)(ws + WS_PO); float* yp = (float*)(ws + WS_YP); unsigned* yf = (unsigned*)(ws + WS_YF);
  u64* ghh = (u64*)(ws + WS_GHH); u64* gop = (u64*)(ws + WS_GOP);
  const unsigned tag = ldw((const unsigned*)(ws + WS_EPOCH)) + 1u;

  const unsigned cpk = reinterpret_cast<const unsigned*>(p.c)[tid];
  const unsigned xpk = reinterpret_cast<const unsigned*>(p.x)[tid];
  const u32x4 wm0 = *reinterpret_cast<const u32x4*>(p.wmod + (size_t)tid * 3072 + sw * 8);
  u32x4 wm1 = u32x4{0, 0, 0, 0};
  if (sw >= 128) wm1 = *reinterpret_cast<const u32x4*>(p.wmod + (size_t)tid * 3072 + (sw + 128) * 8);
  const u32x4 wq0 = *reinterpret_cast<const u32x4*>(p.wq + (size_t)(tid >> 1) * 4096 + sw * 16 + (tid & 1) * 8);
  const u32x4 wq1 = *reinterpret_cast<const u32x4*>(p.wq + (size_t)(256 + (tid >> 1)) * 4096 + sw * 16 + (tid & 1) * 8);
  const int tkg = sw >> 4, tcg = sw & 15;
  const size_t kvbase = ((size_t)(b * HKV + kvh) * SK + (size_t)s * (SK / NSPLIT)) * DH + (size_t)r * DH + 8 * c;
#define ISSUE(KB, VB, i_) do { const bf16* kb_ = p.kc + kvbase + (size_t)(wave + NW * (i_)) * PAGE * DH; const bf16* vb_ = p.vc + kvbase + (size_t)(wave + NW * (i_)) * PAGE * DH; \
    _Pragma("unroll") for (int t_ = 0; t_ < 4; ++t_) { KB[t_] = __builtin_nontemporal_load(reinterpret_cast<const u32x4*>(kb_ + (size_t)t_ * 4 * DH)); VB[t_] = __builtin_nontemporal_load(reinterpret_cast<const u32x4*>(vb_ + (size_t)t_ * 4 * DH)); } } while (0)
  float* pgl = lds + L_PG + wave * 4096;
#define ISSUE_LDS(slot, i_) do { const bf16* kb_ = p.kc + kvbase + (size_t)(wave + NW * (i_)) * PAGE * DH; const bf16* vb_ = p.vc + kvbase + (size_t)(wave + NW * (i_)) * PAGE * DH; \
    _Pragma("unroll") for (int t_ = 0; t_ < 4; ++t_) { GLDS(kb_ + (size_t)t_ * 4 * DH, pgl + (slot) * 2048 + t_ * 256); GLDS(vb_ + (size_t)t_ * 4 * DH, pgl + (slot) * 2048 + 1024 + t_ * 256); } } while (0)
  u32x4 K0[4], V0[4], K1[4], V1[4];
  ISSUE_LDS(0, 0);

  { const float c0 = lo2f(cpk), c1 = hi2f(cpk);
    lds[L_A + 2 * tid] = rbf(c0 / (1.f + __expf(-c0))); lds[L_A + 2 * tid + 1] = rbf(c1 / (1.f + __expf(-c1)));
    lds[L_XS + 2 * tid] = lo2f(xpk); lds[L_XS + 2 * tid + 1] = hi2f(xpk); }
  bar();
  {
    const float a0 = lds[L_A + tid], a1 = lds[L_A + 512 + tid];
    float acc[16];
#pragma unroll
    for (int j = 0; j < 16; ++j) acc[j] = 0.f;
    fma8(acc, wm0, a0, a1);
    float* res = lds + L_CW;
    reduce_tile<1>(acc, lds, tid, res);
    if (tid < 8) { const int bb = tid >> 2, pr = tid & 3; stg(gmod + bb * 1536 + sw * 4 + pr, tag, cvtpk(res[bb * 8 + 2 * pr], res[bb * 8 + 2 * pr + 1])); }
    if (sw >= 128) {
#pragma unroll
      for (int j = 0; j < 16; ++j) acc[j] = 0.f;
      fma8(acc, wm1, a0, a1);
      reduce_tile<1>(acc, lds, tid, res);
      if (tid < 8) { const int bb = tid >> 2, pr = tid & 3; stg(gmod + bb * 1536 + (sw + 128) * 4 + pr, tag, cvtpk(res[bb * 8 + 2 * pr], res[bb * 8 + 2 * pr + 1])); }
    }
  }
  ISSUE_LDS(1, 1);
  {
    unsigned v[2]; sweep<2>(gmod + tid, 1536, tag, v);
#pragma unroll
    for (int bb = 0; bb < 2; ++bb) reinterpret_cast<unsigned*>(lds)[L_MOD + bb * 1536 + tid] = v[bb];
  }
  bar();
  ln_mod(lds, 0, 512, tid);
  {
    float acc[16];
#pragma unroll
    for (int j = 0; j < 16; ++j) acc[j] = 0.f;
    fma8(acc, wq0, lds[L_A + (tid >> 1)], lds[L_A + 512 + (tid >> 1)]);
    fma8(acc, wq1, lds[L_A + 256 + (tid >> 1)], lds[L_A + 512 + 256 + (tid >> 1)]);
    float* res = lds + L_CW;
    reduce_tile<2>(acc, lds, tid, res);
    if (tid < 16) { const int ch = tid >> 3, bb = (tid >> 2) & 1, pr = tid & 3;
      stg(gq + bb * 2048 + sw * 8 + ch * 4 + pr, tag, cvtpk(res[ch * 16 + bb * 8 + 2 * pr], res[ch * 16 + bb * 8 + 2 * pr + 1])); }
  }
  ISSUE(K0, V0, 2);
  { unsigned v[2], w[2];
    sweep<2>(gmod + 512 + tid, 512, tag, v); sweep<2>(gmod + 1536 + 512 + tid, 512, tag, w);
#pragma unroll
    for (int i = 0; i < 2; ++i) { const int pi = 512 + 512 * i + tid;
      reinterpret_cast<unsigned*>(lds)[L_MOD + pi] = v[i]; reinterpret_cast<unsigned*>(lds)[L_MOD + 1536 + pi] = w[i]; } }
  if (tid < 256) { const unsigned v = wait_g(gq + b * 2048 + kvh * 256 + tid, tag); reinterpret_cast<unsigned*>(lds + L_Q)[tid] = v; }
  bar();
  u32x4 qpk[4];
#pragma unroll
  for (int g = 0; g < 4; ++g) qpk[g] = *reinterpret_cast<const u32x4*>(lds + L_Q + g * 64 + 4 * c);

  constexpr float C2 = SCALE * 1.4426950408889634f;
  float m = -INFINITY, l = 0.f, acc[32];
#pragma unroll
  for (int k = 0; k < 32; ++k) acc[k] = 0.f;
  float* prow = lds + L_P + (wave * 4 + r) * 16;
  auto proc = [&](const u32x4 (&Kc)[4], const u32x4 (&Vc)[4]) {
    float x[16];
#pragma unroll
    for (int t = 0; t < 4; ++t)
#pragma unroll
      for (int g = 0; g < 4; ++g) { float d = 0.f;
#pragma unroll
        for (int k = 0; k < 4; ++k) d = dot2(Kc[t][k], qpk[g][k], d);
        x[t * 4 + g] = d; }
#define TR_STEP(HALF, CTRL, BIT) _Pragma("unroll") for (int h = 0; h < (HALF); ++h) { \
      const float lo_ = x[h], hi_ = x[h + (HALF)]; const float t1_ = lo_ + dpp<CTRL>(lo_), t2_ = hi_ + dpp<CTRL>(hi_); \
      x[h] = (c & (BIT)) ? t2_ : t1_; }
    TR_STEP(8, XOR8, 8) TR_STEP(4, XOR7, 4) TR_STEP(2, XOR2, 2) TR_STEP(1, XOR1, 1)
#undef TR_STEP
    const float sc = x[0] * C2;
    float pm = sc; pm = fmaxf(pm, dpp<ROR4>(pm)); pm = fmaxf(pm, dpp<XOR8>(pm)); pm = xrow16_max(pm);
    const float mn = fmaxf(m, pm);
    const float alpha = __builtin_amdgcn_exp2f(m - mn), pp = __builtin_amdgcn_exp2f(sc - mn);
    l = fmaf(l, alpha, pp); m = mn;
#pragma unroll
    for (int q4 = 0; q4 < 4; ++q4) {
      const float al = q4 == 0 ? dpp<0x00>(alpha) : q4 == 1 ? dpp<0x55>(alpha) : q4 == 2 ? dpp<0xAA>(alpha) : dpp<0xFF>(alpha);
#pragma unroll
      for (int k = 0; k < 8; ++k) acc[q4 * 8 + k] *= al; }
    prow[c] = pp;
    asm volatile("s_waitcnt lgkmcnt(0)" ::: "memory");
    f32x4 pr[4];
#pragma unroll
    for (int u = 0; u < 4; ++u) pr[u] = *reinterpret_cast<const f32x4*>(prow + 4 * u);
#pragma unroll
    for (int t = 0; t < 4; ++t) {
      float vf[8];
#pragma unroll
      for (int k = 0; k < 4; ++k) { vf[2 * k] = lo2f(Vc[t][k]); vf[2 * k + 1] = hi2f(Vc[t][k]); }
#pragma unroll
      for (int g = 0; g < 4; ++g) { const float pg_ = pr[t][g];
#pragma unroll
        for (int k = 0; k < 8; ++k) acc[g * 8 + k] = fmaf(pg_, vf[k], acc[g * 8 + k]); } }
  };
  drain();
#pragma unroll 1
  for (int i = 0; i < 2; ++i) {
#pragma unroll
    for (int t = 0; t < 4; ++t) { K1[t] = *reinterpret_cast<const u32x4*>(pgl + i * 2048 + t * 256 + lane * 4); V1[t] = *reinterpret_cast<const u32x4*>(pgl + i * 2048 + 1024 + t * 256 + lane * 4); }
    proc(K1, V1);
  }
  asm volatile("s_waitcnt lgkmcnt(0)" ::: "memory");
  GLDS(p.wo + (size_t)(tkg * 256 + (tid >> 2)) * 512 + tcg * 32 + (tid & 3) * 8, pgl);
  GLDS(p.wo + (size_t)(tkg * 256 + 128 + (tid >> 2)) * 512 + tcg * 32 + (tid & 3) * 8, pgl + 256);
  if (sw < 224) {
    GLDS(p.wg + (size_t)tid * FF + sw * 8, pgl + 512);
    GLDS(p.wu + (size_t)tid * FF + sw * 8, pgl + 768);
    GLDS(p.wd + (size_t)(tkg * 128 + (tid >> 2)) * 512 + tcg * 32 + (tid & 3) * 8, pgl + 1024);
  }
#pragma unroll 1
  for (int i = 2; i < PPW; i += 2) {
    ISSUE(K1, V1, i + 1); __builtin_amdgcn_sched_barrier(0);
    proc(K0, V0); __builtin_amdgcn_sched_barrier(0);
    if (i + 2 < PPW) ISSUE(K0, V0, i + 2);
    __builtin_amdgcn_sched_barrier(0);
    proc(K1, V1); __builtin_amdgcn_sched_barrier(0);
  }
#undef ISSUE
#undef ISSUE_LDS
#pragma unroll
  for (int k = 0; k < 32; ++k) acc[k] = xrow16_sum(acc[k]);
  { float t = l; t += dpp<ROR4>(t); t += dpp<XOR8>(t); l = xrow16_sum(t); }
  float* mrg = lds + L_PG + 2048;
  if (r == 0) {
#pragma unroll
    for (int g = 0; g < 4; ++g) {
      float* rec = mrg + wave * 4096 + g * 132;
#pragma unroll
      for (int k = 0; k < 8; k += 4) *reinterpret_cast<f32x4*>(rec + 4 + 8 * c + k) = f32x4{acc[g * 8 + k], acc[g * 8 + k + 1], acc[g * 8 + k + 2], acc[g * 8 + k + 3]};
      if (c == g) { rec[0] = m; rec[1] = l; } } }
  bar();
  {
    const int g = tid >> 7, d = tid & 127;
    float M = -INFINITY;
#pragma unroll
    for (int w = 0; w < NW; ++w) M = fmaxf(M, mrg[w * 4096 + g * 132]);
    float L = 0.f, o = 0.f;
#pragma unroll
    for (int w = 0; w < NW; ++w) { const float* rec = mrg + w * 4096 + g * 132; const float wt = __builtin_amdgcn_exp2f(rec[0] - M);
      L = fmaf(wt, rec[1], L); o = fmaf(wt, rec[4 + d], o); }
    stf(po + (size_t)wg * 512 + tid, o);
    drain(); bar();
    if (d == 0) { stg(gml + (wg * 4 + g) * 2, tag, __float_as_uint(M)); stg(gml + (wg * 4 + g) * 2 + 1, tag, __float_as_uint(L)); }
  }
  {
    if (tid < 64) {
      const int bb = tid >> 5, hh = (tid >> 4) & 1, ss = tid & 15;
      const int h = tkg * 2 + hh, src = (bb * 8 + (h >> 2)) * 16 + ss;
      unsigned v[2]; sweep<2>(gml + (src * 4 + (h & 3)) * 2, 1, tag, v);
      const float ms = __uint_as_float(v[0]), ls = __uint_as_float(v[1]);
      float M = ms; M = fmaxf(M, dpp<XOR1>(M)); M = fmaxf(M, dpp<XOR2>(M)); M = fmaxf(M, dpp<XOR7>(M)); M = fmaxf(M, dpp<XOR8>(M));
      const float w = __builtin_amdgcn_exp2f(ms - M);
      float L = w * ls; L += dpp<XOR1>(L); L += dpp<XOR2>(L); L += dpp<XOR7>(L); L += dpp<XOR8>(L);
      lds[L_CW + tid] = w / L;
    }
    bar();
    { const int bb = tid >> 8, hh = (tid >> 7) & 1, d = tid & 127, h = tkg * 2 + hh;
      const float* src = po + ((size_t)(bb * 8 + (h >> 2)) * 16 * 4 + (h & 3)) * 128 + d;
      float v[16];
#pragma unroll
      for (int ss = 0; ss < 16; ++ss) v[ss] = ldf(src + (size_t)ss * 512);
      float o = 0.f;
#pragma unroll
      for (int ss = 0; ss < 16; ++ss) o = fmaf(lds[L_CW + (bb * 2 + hh) * 16 + ss], v[ss], o);
      lds[L_A + bb * 256 + hh * 128 + d] = rbf(o);
    }
    bar();
    float ac[16];
#pragma unroll
    for (int j = 0; j < 16; ++j) ac[j] = 0.f;
    const u32x4* wl = reinterpret_cast<const u32x4*>(pgl) + lane;
    fma8(ac, wl[0], lds[L_A + (tid >> 2)], lds[L_A + 256 + (tid >> 2)]);
    fma8(ac, wl[64], lds[L_A + 128 + (tid >> 2)], lds[L_A + 256 + 128 + (tid >> 2)]);
    float* res = lds + L_CW;
    reduce_tile<4>(ac, lds, tid, res);
    if (wave == 0) {
      { const int ch = tid >> 4, bb = (tid >> 3) & 1, j = tid & 7; stf(yp + (tkg * 2 + bb) * 512 + tcg * 32 + ch * 8 + j, res[tid]); }
      drain();
      if (tid == 0) stw(yf + sw, tag);
    }
  }
  if (sw >= 224) return;
  {
    if (tid < 256) wait_w(yf + tid, tag);
    bar();
#pragma unroll
    for (int ii = 0; ii < 2; ++ii) { const int i = tid + 512 * ii;
      float v[16];
#pragma unroll
      for (int k = 0; k < 16; ++k) v[k] = ldf(yp + k * 1024 + i);
      float y = 0.f;
#pragma unroll
      for (int k = 0; k < 16; ++k) y += v[k];
      lds[L_XS + i] = rbf(lds[L_XS + i] + rbf(modv(lds, ii, 1024 + tid) * rbf(y)));
    }
    bar();
    ln_mod(lds, 1536, 2048, tid);
    const u32x4* wl = reinterpret_cast<const u32x4*>(pgl) + lane;
    const float a0 = lds[L_A + tid], a1 = lds[L_A + 512 + tid];
    float ac[16];
#pragma unroll
    for (int j = 0; j < 16; ++j) ac[j] = 0.f;
    fma8(ac, wl[128], a0, a1);
    float* res = lds + L_CW; float* res2 = lds + L_CW + 16;
    reduce_tile<1>(ac, lds, tid, res);
#pragma unroll
    for (int j = 0; j < 16; ++j) ac[j] = 0.f;
    fma8(ac, wl[192], a0, a1);
    reduce_tile<1>(ac, lds, tid, res2);
    if (tid < 8) { const int bb = tid >> 2, pr = tid & 3;
      const float h0 = rbf(res[bb * 8 + 2 * pr]) * rbf(res2[bb * 8 + 2 * pr]), h1 = rbf(res[bb * 8 + 2 * pr + 1]) * rbf(res2[bb * 8 + 2 * pr + 1]);
      stg(ghh + bb * 896 + sw * 4 + pr, tag, cvtpk(h0, h1)); }
  }
  {
    if (tid < 128) { const int bb = tid >> 6, pi = tid & 63; const unsigned v = wait_g(ghh + bb * 896 + tkg * 64 + pi, tag);
      lds[L_A + bb * 128 + 2 * pi] = lo2f(v); lds[L_A + bb * 128 + 2 * pi + 1] = hi2f(v); }
    bar();
    const u32x4* wl = reinterpret_cast<const u32x4*>(pgl) + lane;
    float ac[16];
#pragma unroll
    for (int j = 0; j < 16; ++j) ac[j] = 0.f;
    fma8(ac, wl[256], lds[L_A + (tid >> 2)], lds[L_A + 128 + (tid >> 2)]);
    float* res = lds + L_CW;
    reduce_tile<4>(ac, lds, tid, res);
    if (tid < 64) { const int ch = tid >> 4, bb = (tid >> 3) & 1, j = tid & 7; stg(gop + (tkg * 2 + bb) * 512 + tcg * 32 + ch * 8 + j, tag, __float_as_uint(res[tid])); }
  }
  if (tkg == 0 && tid < 64) {
    const int bb = tid >> 5, cc = tid & 31, n = tcg * 32 + cc;
    unsigned v[14]; sweep<14>(gop + bb * 512 + n, 1024, tag, v);
    float y = 0.f;
#pragma unroll
    for (int k = 0; k < 14; ++k) y += __uint_as_float(v[k]);
    const float o = lds[L_XS + bb * 512 + n] + rbf(modv(lds, bb, 2560 + n) * rbf(y));
    p.out[bb * 512 + n] = (unsigned short)cvtpk(o, 0.f);
    if (sw == 0 && tid == 0) stw((unsigned*)(ws + WS_EPOCH), tag);
  }
}

extern "C" void kernel_launch(void* const* d_in, const int* in_sizes, int n_in,
                              void* d_out, int out_size, void* d_ws, size_t ws_size,
                              hipStream_t stream) {
  Params p;
  p.x = (const bf16*)d_in[0]; p.kc = (const bf16*)d_in[1]; p.vc = (const bf16*)d_in[2];
  p.wq = (const bf16*)d_in[3]; p.wo = (const bf16*)d_in[4];
  p.wg = (const bf16*)d_in[6]; p.wu = (const bf16*)d_in[7]; p.wd = (const bf16*)d_in[8];
  p.c = (const bf16*)d_in[9]; p.wmod = (const bf16*)d_in[10];
  p.out = (unsigned short*)d_out; p.ws = (char*)d_ws;
  mega<<<NWG, NT, 0, stream>>>(p);
}
```

```cpp
#include <hip/hip_runtime.h>
#include <hip/hip_bf16.h>
#include <cstdint>

using bf16 = __hip_bfloat16;
using u64 = unsigned long long;
using u32x4  = __attribute__((ext_vector_type(4))) unsigned;
using f32x4  = __attribute__((ext_vector_type(4))) float;
using bf16x2 = __attribute__((ext_vector_type(2))) __bf16;
using s16x8 = __attribute__((ext_vector_type(8))) short;

constexpr int HQ = 32, HKV = 8, SK = 32768, DH = 128, FF = 1792;
constexpr int NSPLIT = 16, NWG = 256, NT = 512, NW = 8;
constexpr int PAGE = 16, NPAGE = SK / PAGE;
constexpr int NSTAT = 14 * NW * 4;
constexpr float SCALE = 0.088388347648318440f;
constexpr unsigned SPIN_CAP = 1u << 22;

constexpr size_t WS_EPOCH = 0;
constexpr size_t WS_CNT  = 1024;
constexpr size_t WS_GMOD = 4096;
constexpr size_t WS_GQ   = WS_GMOD + 2 * 1536 * 8;
constexpr size_t WS_GML  = WS_GQ + 2 * 2048 * 8;
constexpr size_t WS_PO   = WS_GML + 256 * 8 * 8;
constexpr size_t WS_YP   = WS_PO + 256 * 512 * 8;
constexpr size_t WS_GHH  = WS_YP + 16 * 1024 * 8;
constexpr size_t WS_GOP  = WS_GHH + 2 * 896 * 8;
constexpr size_t WS_GQP  = WS_GOP + 14 * 1024 * 8;
constexpr size_t WS_GMP  = WS_GQP + 16 * 8192 * 8;

#define ST(k) do {} while (0)
struct Params {
  const bf16 *x, *kc, *vc, *wq, *wo, *wg, *wu, *wd, *c, *wmod;
  unsigned short* out; char* ws;
};

__device__ __forceinline__ float lo2f(unsigned u) { return __builtin_bit_cast(float, u << 16); }
__device__ __forceinline__ float hi2f(unsigned u) { return __builtin_bit_cast(float, u & 0xffff0000u); }
__device__ __forceinline__ unsigned cvtpk(float lo, float hi) {
  unsigned r; asm volatile("v_cvt_pk_bf16_f32 %0, %1, %2" : "=v"(r) : "v"(lo), "v"(hi)); return r;
}
__device__ __forceinline__ float rbf(float x) { return lo2f(cvtpk(x, x)); }
__device__ __forceinline__ float dot2(unsigned k, unsigned q, float acc) {
  return __builtin_amdgcn_fdot2_f32_bf16(__builtin_bit_cast(bf16x2, k), __builtin_bit_cast(bf16x2, q), acc, false);
}
template <int CTRL> __device__ __forceinline__ float dpp(float x) {
  return __builtin_bit_cast(float, __builtin_amdgcn_mov_dpp(__builtin_bit_cast(int, x), CTRL, 0xf, 0xf, true));
}
constexpr int XOR1 = 0xB1, XOR2 = 0x4E, XOR7 = 0x141, XOR8 = 0x128, ROR4 = 0x124;
__device__ __forceinline__ float xrow16_max(float x) {
  auto s = __builtin_amdgcn_permlane16_swap(__float_as_uint(x), __float_as_uint(x), false, false);
  x = fmaxf(__uint_as_float(s[0]), __uint_as_float(s[1]));
  auto t = __builtin_amdgcn_permlane32_swap(__float_as_uint(x), __float_as_uint(x), false, false);
  return fmaxf(__uint_as_float(t[0]), __uint_as_float(t[1]));
}
__device__ __forceinline__ float xrow16_sum(float x) {
  auto s = __builtin_amdgcn_permlane16_swap(__float_as_uint(x), __float_as_uint(x), false, false);
  x = __uint_as_float(s[0]) + __uint_as_float(s[1]);
  auto t = __builtin_amdgcn_permlane32_swap(__float_as_uint(x), __float_as_uint(x), false, false);
  return __uint_as_float(t[0]) + __uint_as_float(t[1]);
}
__device__ __forceinline__ float wave_sum(float x) {
  x += dpp<XOR1>(x); x += dpp<XOR2>(x); x += dpp<XOR7>(x); x += dpp<XOR8>(x); return xrow16_sum(x);
}
__device__ __forceinline__ void bar() { asm volatile("s_waitcnt lgkmcnt(0)\n\ts_barrier" ::: "memory"); }
__device__ __forceinline__ void drain() { asm volatile("s_waitcnt vmcnt(0)" ::: "memory"); }
__device__ __forceinline__ u64 ldg(const u64* p) { return __hip_atomic_load(p, __ATOMIC_RELAXED, __HIP_MEMORY_SCOPE_AGENT); }
__device__ __forceinline__ void stg(u64* p, unsigned tag, unsigned v) { __hip_atomic_store(p, ((u64)tag << 32) | v, __ATOMIC_RELAXED, __HIP_MEMORY_SCOPE_AGENT); }
__device__ __forceinline__ unsigned ldw(const unsigned* p) { return __hip_atomic_load(p, __ATOMIC_RELAXED, __HIP_MEMORY_SCOPE_AGENT); }
__device__ __forceinline__ void stw(unsigned* p, unsigned v) { __hip_atomic_store(p, v, __ATOMIC_RELAXED, __HIP_MEMORY_SCOPE_AGENT); }
template <int N> __device__ __forceinline__ void sweep(const u64* g, int stride, unsigned tag, unsigned (&v)[N]) {
  for (unsigned sp = 0; sp < SPIN_CAP; ++sp) {
    u64 x[N];
#pragma unroll
    for (int k = 0; k < N; ++k) x[k] = ldg(g + (size_t)k * stride);
    bool ok = true;
#pragma unroll
    for (int k = 0; k < N; ++k) { ok = ok && ((unsigned)(x[k] >> 32) == tag); v[k] = (unsigned)x[k]; }
    if (ok) break;
    __builtin_amdgcn_s_sleep(1);
  }
}
__device__ __forceinline__ void prepoll(const u64* g, unsigned tag) {
  for (unsigned sp = 0; sp < SPIN_CAP; ++sp) { if ((unsigned)(ldg(g) >> 32) == tag) break; __builtin_amdgcn_s_sleep(1); }
}
__device__ __forceinline__ void fma8(float (&acc)[16], u32x4 w, float a0, float a1) {
#pragma unroll
  for (int j = 0; j < 4; ++j) {
    const float l = lo2f(w[j]), h = hi2f(w[j]);
    acc[2 * j] = fmaf(a0, l, acc[2 * j]); acc[2 * j + 1] = fmaf(a0, h, acc[2 * j + 1]);
    acc[8 + 2 * j] = fmaf(a1, l, acc[8 + 2 * j]); acc[8 + 2 * j + 1] = fmaf(a1, h, acc[8 + 2 * j + 1]);
  }
}

constexpr int L_PG = 0;
constexpr int L_A = 0;
constexpr int L_XS = L_A + 1024;
constexpr int L_MOD = L_XS + 1024;
constexpr int L_P = L_MOD + 3072;
constexpr int L_Q = L_P + 512;
constexpr int L_RED = L_Q + 256;
constexpr int L_RES = L_RED + 1024;
constexpr int L_ACT = L_RES + 128;
constexpr int L_MODT = L_ACT + 512;
constexpr int L_ST = L_MODT + 512;
constexpr int L_END = L_ST;
#define GLDS(src, dst) __builtin_amdgcn_global_load_lds((const unsigned*)(src), (unsigned*)(dst), 16, 0, 2)
__device__ __forceinline__ float modv(const float* lds, int b, int col) {
  const unsigned v = reinterpret_cast<const unsigned*>(lds)[L_MOD + b * 1536 + (col >> 1)]; return (col & 1) ? hi2f(v) : lo2f(v); }

template <int NCH>
__device__ __forceinline__ void reduce_tile(float (&acc)[16], float* lds, int tid) {
  const int lane = tid & 63, wave = tid >> 6;
#pragma unroll
  for (int j = 0; j < 16; ++j) {
    float v = acc[j];
    if (NCH == 1) { v += dpp<XOR1>(v); v += dpp<XOR2>(v); }
    if (NCH == 2) { v += dpp<XOR2>(v); }
    if (NCH <= 4) { v += dpp<XOR8>(v); v += dpp<ROR4>(v); }
    if (NCH == 8) { v += dpp<XOR8>(v); }
    acc[j] = xrow16_sum(v);
  }
  float* part = lds + L_RED;
  if (lane < NCH) {
#pragma unroll
    for (int j = 0; j < 16; j += 4) *reinterpret_cast<f32x4*>(part + (wave * NCH + lane) * 16 + j) = f32x4{acc[j], acc[j + 1], acc[j + 2], acc[j + 3]};
  }
  bar();
  if (tid < NCH * 16) { float s = 0.f;
#pragma unroll
    for (int w = 0; w < 8; ++w) s += part[w * NCH * 16 + tid];
    lds[L_RES + tid] = s; }
  bar();
}

__device__ __forceinline__ void ln_mod(float* lds, int soff, int coff, int tid) {
  const int lane = tid & 63, wave = tid >> 6;
  if (wave < 2) {
    const float* x = lds + L_XS + wave * 512;
    float xv[8]; float s = 0.f;
#pragma unroll
    for (int i = 0; i < 8; ++i) { xv[i] = x[lane + 64 * i]; s += xv[i]; }
    s = wave_sum(s) * (1.f / 512.f);
    const float m = rbf(s);
    float v = 0.f;
#pragma unroll
    for (int i = 0; i < 8; ++i) { const float d = xv[i] - s; v += d * d; }
    v = rbf(wave_sum(v) * (1.f / 512.f));
    const float den = rbf(sqrtf(rbf(v + 1e-5f)));
#pragma unroll
    for (int i = 0; i < 8; ++i) {
      const float n = rbf(rbf(xv[i] - m) / den);
      lds[L_A + wave * 512 + lane + 64 * i] = rbf(rbf(n * rbf(1.f + modv(lds, wave, coff + lane + 64 * i))) + modv(lds, wave, soff + lane + 64 * i));
    }
  }
  bar();
}
__device__ __forceinline__ void ln_mod_all(float* lds, int soff, int coff, int tid) {
  const int lane = tid & 63;
  float s0 = 0.f, s1 = 0.f, x0[8], x1[8];
#pragma unroll
  for (int i = 0; i < 8; ++i) { x0[i] = lds[L_XS + lane + 64 * i]; x1[i] = lds[L_XS + 512 + lane + 64 * i]; s0 += x0[i]; s1 += x1[i]; }
  s0 = wave_sum(s0) * (1.f / 512.f); s1 = wave_sum(s1) * (1.f / 512.f);
  float v0 = 0.f, v1 = 0.f;
#pragma unroll
  for (int i = 0; i < 8; ++i) { const float d0 = x0[i] - s0, d1 = x1[i] - s1; v0 = fmaf(d0, d0, v0); v1 = fmaf(d1, d1, v1); }
  v0 = rbf(wave_sum(v0) * (1.f / 512.f)); v1 = rbf(wave_sum(v1) * (1.f / 512.f));
  const float den0 = rbf(sqrtf(rbf(v0 + 1e-5f))), den1 = rbf(sqrtf(rbf(v1 + 1e-5f)));
  const float n0 = rbf(rbf(lds[L_XS + tid] - rbf(s0)) / den0), n1 = rbf(rbf(lds[L_XS + 512 + tid] - rbf(s1)) / den1);
  const float h0 = rbf(n0 * rbf(1.f + modv(lds, 0, coff + tid))) + modv(lds, 0, soff + tid);
  const float h1 = rbf(n1 * rbf(1.f + modv(lds, 1, coff + tid))) + modv(lds, 1, soff + tid);
  unsigned short* act = reinterpret_cast<unsigned short*>(lds + L_ACT);
  const unsigned pk = cvtpk(h0, h1);
  act[tid] = (unsigned short)pk; act[512 + tid] = (unsigned short)(pk >> 16);
  bar();
}

__global__ void __launch_bounds__(NT, 2) mega(Params p) {
  __shared__ __attribute__((aligned(16))) float lds[L_END];
  __shared__ __attribute__((aligned(16))) float ldsP[32768];
  const int wg = blockIdx.x, tid = threadIdx.x, lane = tid & 63, wave = __builtin_amdgcn_readfirstlane(tid >> 6);
  const int sw = (wg & 7) * 32 + (wg >> 3);
  const int r = lane >> 4, c = lane & 15;
  const int pair = wg >> 4, s = wg & 15, b = pair >> 3, kvh = pair & 7;
  char* ws = p.ws;
  u64* gmod = (u64*)(ws + WS_GMOD); u64* gq = (u64*)(ws + WS_GQ); u64* gml = (u64*)(ws + WS_GML);
  u64* po = (u64*)(ws + WS_PO); u64* yp = (u64*)(ws + WS_YP);
  u64* ghh = (u64*)(ws + WS_GHH); u64* gop = (u64*)(ws + WS_GOP);
  ST(0);
  const int tkg = sw >> 4, tcg = sw & 15;
  const size_t kvbase = (size_t)(b * HKV + kvh) * SK * DH + (size_t)r * DH + 8 * c;
  float* pgl = ldsP + wave * 4096;
#define ISSUE_LDS(slot, pg_) do { const bf16* kb_ = p.kc + kvbase + (size_t)(pg_) * PAGE * DH; const bf16* vb_ = p.vc + kvbase + (size_t)(pg_) * PAGE * DH; \
    _Pragma("unroll") for (int t_ = 0; t_ < 4; ++t_) { GLDS(kb_ + (size_t)t_ * 4 * DH, pgl + (slot) * 2048 + t_ * 256); GLDS(vb_ + (size_t)t_ * 4 * DH, pgl + (slot) * 2048 + 1024 + t_ * 256); } } while (0)
#define ISSUE(KB, VB, pg_) do { const bf16* kb_ = p.kc + kvbase + (size_t)(pg_) * PAGE * DH; const bf16* vb_ = p.vc + kvbase + (size_t)(pg_) * PAGE * DH; \
    _Pragma("unroll") for (int t_ = 0; t_ < 4; ++t_) { KB[t_] = __builtin_nontemporal_load(reinterpret_cast<const u32x4*>(kb_ + (size_t)t_ * 4 * DH)); VB[t_] = __builtin_nontemporal_load(reinterpret_cast<const u32x4*>(vb_ + (size_t)t_ * 4 * DH)); } } while (0)
  u32x4 K0[4], V0[4], K1[4], V1[4];
  const int mkg = sw >> 6, mcg = sw & 63;
  const int qkg = sw >> 6, qcg = sw & 63;
  const unsigned xpk = reinterpret_cast<const unsigned*>(p.x)[tid];
  const unsigned cv = reinterpret_cast<const unsigned short*>(p.c)[((tid >> 7) & 1) * 512 + mkg * 128 + (tid & 127)];
  const unsigned tag = ldw((const unsigned*)(ws + WS_EPOCH)) + 1u;
  u32x4 wm = u32x4{0, 0, 0, 0};
  if (tid < 256) wm = *reinterpret_cast<const u32x4*>(p.wmod + (size_t)(mkg * 128 + (tid >> 1)) * 3072 + mcg * 16 + (tid & 1) * 8);
  const u32x4 wq0 = *reinterpret_cast<const u32x4*>(p.wq + (size_t)(qkg * 128 + lane) * 4096 + qcg * 64 + wave * 8);
  const u32x4 wq1 = *reinterpret_cast<const u32x4*>(p.wq + (size_t)(qkg * 128 + 64 + lane) * 4096 + qcg * 64 + wave * 8);
  const int pj = s * 128 + wave;
  ISSUE(K0, V0, pj);
  lds[L_XS + 2 * tid] = lo2f(xpk); lds[L_XS + 2 * tid + 1] = hi2f(xpk);
  if (tid < 256) { const float c0 = lo2f(cv); lds[L_A + tid] = rbf(c0 / (1.f + __expf(-c0))); }
  bar();
  float mean0, mean1, den0, den1;
  { float s0 = 0.f, s1 = 0.f, x0[8], x1[8];
#pragma unroll
    for (int i = 0; i < 8; ++i) { x0[i] = lds[L_XS + lane + 64 * i]; x1[i] = lds[L_XS + 512 + lane + 64 * i]; s0 += x0[i]; s1 += x1[i]; }
    s0 = wave_sum(s0) * (1.f / 512.f); s1 = wave_sum(s1) * (1.f / 512.f);
    float v0 = 0.f, v1 = 0.f;
#pragma unroll
    for (int i = 0; i < 8; ++i) { const float d0 = x0[i] - s0, d1 = x1[i] - s1; v0 = fmaf(d0, d0, v0); v1 = fmaf(d1, d1, v1); }
    v0 = rbf(wave_sum(v0) * (1.f / 512.f)); v1 = rbf(wave_sum(v1) * (1.f / 512.f));
    mean0 = rbf(s0); mean1 = rbf(s1); den0 = rbf(sqrtf(rbf(v0 + 1e-5f))); den1 = rbf(sqrtf(rbf(v1 + 1e-5f))); }
  {
    float acc[16];
#pragma unroll
    for (int jj = 0; jj < 16; ++jj) acc[jj] = 0.f;
    if (tid < 256) fma8(acc, wm, lds[L_A + (tid >> 1)], lds[L_A + 128 + (tid >> 1)]);
    reduce_tile<2>(acc, lds, tid);
    if (tid < 32) { const int ch = tid >> 4, bb = (tid >> 3) & 1, j = tid & 7;
      stg((u64*)(ws + WS_GMP) + (mkg * 2 + bb) * 1024 + mcg * 16 + ch * 8 + j, tag, __float_as_uint(lds[L_RES + tid])); }
  }
  ST(1);
  drain(); ISSUE(K1, V1, pj + 8);
  { const int bb = tid >> 8, wh = (tid >> 7) & 1, kk = tid & 127; unsigned v[4];
    sweep<4>((const u64*)(ws + WS_GMP) + bb * 1024 + wh * 512 + qkg * 128 + kk, 2048, tag, v);
    lds[L_MODT + tid] = rbf((__uint_as_float(v[0]) + __uint_as_float(v[1])) + (__uint_as_float(v[2]) + __uint_as_float(v[3]))); }
  bar();
  ST(2);
  if (tid < 256) { const int bb = tid >> 7, kk = tid & 127;
    const float sa = lds[L_MODT + bb * 256 + kk], ca = lds[L_MODT + bb * 256 + 128 + kk];
    const float xx = lds[L_XS + bb * 512 + qkg * 128 + kk];
    const float n = rbf(rbf(xx - (bb ? mean1 : mean0)) / (bb ? den1 : den0));
    lds[L_A + 256 + tid] = rbf(rbf(n * rbf(1.f + ca)) + sa); }
  bar();
  {
    float acc[16];
#pragma unroll
    for (int jj = 0; jj < 16; ++jj) acc[jj] = 0.f;
    fma8(acc, wq0, lds[L_A + 256 + lane], lds[L_A + 256 + 128 + lane]);
    fma8(acc, wq1, lds[L_A + 256 + 64 + lane], lds[L_A + 256 + 128 + 64 + lane]);
    float val = 0.f;
#pragma unroll
    for (int jj = 0; jj < 16; ++jj) { const float v = wave_sum(acc[jj]); val = (lane == jj) ? v : val; }
    if (lane < 16) stg((u64*)(ws + WS_GQP) + (qkg * 2 + (lane >> 3)) * 4096 + qcg * 64 + wave * 8 + (lane & 7), tag, __float_as_uint(val));
  }
  ST(3);
  drain(); ISSUE_LDS(0, pj + 16);
  { unsigned v[4];
    sweep<4>((const u64*)(ws + WS_GQP) + b * 4096 + kvh * 512 + tid, 8192, tag, v);
    const float q = (__uint_as_float(v[0]) + __uint_as_float(v[1])) + (__uint_as_float(v[2]) + __uint_as_float(v[3]));
    const float qn = dpp<XOR1>(q);
    if ((tid & 1) == 0) reinterpret_cast<unsigned*>(lds + L_Q)[tid >> 1] = cvtpk(q, qn); }
  drain(); ISSUE_LDS(1, pj + 24);
  bar();
  ST(4);
  u32x4 qpk[4];
#pragma unroll
  for (int g = 0; g < 4; ++g) qpk[g] = *reinterpret_cast<const u32x4*>(lds + L_Q + g * 64 + 4 * c);

  constexpr float C2 = SCALE * 1.4426950408889634f;
  float m = -INFINITY, l = 0.f, acc[32];
#pragma unroll
  for (int k = 0; k < 32; ++k) acc[k] = 0.f;
  float* prow = lds + L_P + (wave * 4 + r) * 16;
  auto proc = [&](const u32x4 (&Kc)[4], const u32x4 (&Vc)[4]) {
    float x[16];
#pragma unroll
    for (int t = 0; t < 4; ++t)
#pragma unroll
      for (int g = 0; g < 4; ++g) { float d = 0.f;
#pragma unroll
        for (int k = 0; k < 4; ++k) d = dot2(Kc[t][k], qpk[g][k], d);
        x[t * 4 + g] = d; }
#define TR_STEP(HALF, CTRL, BIT) _Pragma("unroll") for (int h = 0; h < (HALF); ++h) { \
      const float lo_ = x[h], hi_ = x[h + (HALF)]; const float t1_ = lo_ + dpp<CTRL>(lo_), t2_ = hi_ + dpp<CTRL>(hi_); \
      x[h] = (c & (BIT)) ? t2_ : t1_; }
    TR_STEP(8, XOR8, 8) TR_STEP(4, XOR7, 4) TR_STEP(2, XOR2, 2) TR_STEP(1, XOR1, 1)
#undef TR_STEP
    const float sc = x[0] * C2;
    float pm = sc; pm = fmaxf(pm, dpp<ROR4>(pm)); pm = fmaxf(pm, dpp<XOR8>(pm)); pm = xrow16_max(pm);
    const float mn = fmaxf(m, pm);
    const float alpha = __builtin_amdgcn_exp2f(m - mn), pp = __builtin_amdgcn_exp2f(sc - mn);
    l = fmaf(l, alpha, pp); m = mn;
#pragma unroll
    for (int q4 = 0; q4 < 4; ++q4) {
      const float al = q4 == 0 ? dpp<0x00>(alpha) : q4 == 1 ? dpp<0x55>(alpha) : q4 == 2 ? dpp<0xAA>(alpha) : dpp<0xFF>(alpha);
#pragma unroll
      for (int k = 0; k < 8; ++k) acc[q4 * 8 + k] *= al; }
    prow[c] = pp;
    asm volatile("s_waitcnt lgkmcnt(0)" ::: "memory");
    f32x4 pr[4];
#pragma unroll
    for (int u = 0; u < 4; ++u) pr[u] = *reinterpret_cast<const f32x4*>(prow + 4 * u);
#pragma unroll
    for (int t = 0; t < 4; ++t) {
      float vf[8];
#pragma unroll
      for (int k = 0; k < 4; ++k) { vf[2 * k] = lo2f(Vc[t][k]); vf[2 * k + 1] = hi2f(Vc[t][k]); }
#pragma unroll
      for (int g = 0; g < 4; ++g) { const float pg_ = pr[t][g];
#pragma unroll
        for (int k = 0; k < 8; ++k) acc[g * 8 + k] = fmaf(pg_, vf[k], acc[g * 8 + k]); } }
  };
  proc(K0, V0); __builtin_amdgcn_sched_barrier(0); ISSUE(K0, V0, pj + 4 * 8); __builtin_amdgcn_sched_barrier(0);
  proc(K1, V1); __builtin_amdgcn_sched_barrier(0);
  {
    asm volatile("s_waitcnt vmcnt(8)" ::: "memory");
    ISSUE(K1, V1, pj + 5 * 8);
#pragma unroll 1
    for (int i = 0; i < 2; ++i) {
      u32x4 Kt[4], Vt[4];
#pragma unroll
      for (int t = 0; t < 4; ++t) { Kt[t] = *reinterpret_cast<const u32x4*>(pgl + i * 2048 + t * 256 + lane * 4); Vt[t] = *reinterpret_cast<const u32x4*>(pgl + i * 2048 + 1024 + t * 256 + lane * 4); }
      proc(Kt, Vt);
    }
  }
  const u32x4 wm2 = *reinterpret_cast<const u32x4*>(p.wmod + (size_t)tid * 3072 + 1024 + sw * 8);
  const unsigned cpk2a = reinterpret_cast<const unsigned short*>(p.c)[tid], cpk2b = reinterpret_cast<const unsigned short*>(p.c)[512 + tid];
  { float acc[16];
#pragma unroll
    for (int jj = 0; jj < 16; ++jj) acc[jj] = 0.f;
    const float c0 = lo2f(cpk2a), c1 = lo2f(cpk2b);
    fma8(acc, wm2, rbf(c0 / (1.f + __expf(-c0))), rbf(c1 / (1.f + __expf(-c1))));
    reduce_tile<1>(acc, lds, tid);
    if (tid < 8) { const int bb = tid >> 2, pr = tid & 3;
      stg(gmod + bb * 1536 + 512 + sw * 4 + pr, tag, cvtpk(lds[L_RES + bb * 8 + 2 * pr], lds[L_RES + bb * 8 + 2 * pr + 1])); } }
  {
    asm volatile("s_waitcnt lgkmcnt(0)" ::: "memory");
    unsigned short* wt = reinterpret_cast<unsigned short*>(pgl);
    u32x4 t0 = *reinterpret_cast<const u32x4*>(p.wo + (size_t)(tkg * 256 + wave * 32 + (lane >> 2)) * 512 + tcg * 32 + (lane & 3) * 8);
    u32x4 t1 = *reinterpret_cast<const u32x4*>(p.wo + (size_t)(tkg * 256 + wave * 32 + 16 + (lane >> 2)) * 512 + tcg * 32 + (lane & 3) * 8);
    u32x4 t2 = u32x4{0, 0, 0, 0}, t3 = t2, t4 = t2;
    if (sw < 224) {
      t2 = *reinterpret_cast<const u32x4*>(p.wg + (size_t)(wave * 64 + lane) * FF + sw * 8);
      t3 = *reinterpret_cast<const u32x4*>(p.wu + (size_t)(wave * 64 + lane) * FF + sw * 8);
      t4 = *reinterpret_cast<const u32x4*>(p.wd + (size_t)(tkg * 128 + (wave >> 1) * 32 + (lane >> 1)) * 512 + tcg * 32 + (wave & 1) * 16 + (lane & 1) * 8);
    }
#pragma unroll
    for (int e = 0; e < 8; ++e) {
      const int sh = (e & 1) * 16, ix = e >> 1;
      const int n = (lane & 3) * 8 + e;
      wt[(n >> 4) * 512 + (n & 15) * 32 + (lane >> 2)] = (unsigned short)(t0[ix] >> sh);
      wt[(n >> 4) * 512 + (n & 15) * 32 + 16 + (lane >> 2)] = (unsigned short)(t1[ix] >> sh);
      wt[1024 + e * 64 + lane] = (unsigned short)(t2[ix] >> sh);
      wt[1024 + (8 + e) * 64 + lane] = (unsigned short)(t3[ix] >> sh);
      wt[2048 + ((lane & 1) * 8 + e) * 32 + (lane >> 1)] = (unsigned short)(t4[ix] >> sh);
    }
  }
#pragma unroll 1
  for (int i = 4; i < 16; i += 2) {
    proc(K0, V0); __builtin_amdgcn_sched_barrier(0);
    if (i + 2 < 16) ISSUE(K0, V0, pj + (i + 2) * 8);
    __builtin_amdgcn_sched_barrier(0);
    proc(K1, V1); __builtin_amdgcn_sched_barrier(0);
    if (i + 3 < 16) ISSUE(K1, V1, pj + (i + 3) * 8);
    __builtin_amdgcn_sched_barrier(0);
  }
#undef ISSUE
#undef ISSUE_LDS
  ST(5);
  { unsigned v[2], w[2];
    sweep<2>(gmod + 512 + tid, 512, tag, v); sweep<2>(gmod + 1536 + 512 + tid, 512, tag, w);
#pragma unroll
    for (int i = 0; i < 2; ++i) { const int pi = 512 + 512 * i + tid;
      reinterpret_cast<unsigned*>(lds)[L_MOD + pi] = v[i]; reinterpret_cast<unsigned*>(lds)[L_MOD + 1536 + pi] = w[i]; } }
#pragma unroll
  for (int k = 0; k < 32; ++k) acc[k] = xrow16_sum(acc[k]);
  { float t = l; t += dpp<ROR4>(t); t += dpp<XOR8>(t); l = xrow16_sum(t); }
  float* mrg = ldsP + 2048;
  if (r == 0) {
#pragma unroll
    for (int g = 0; g < 4; ++g) {
      float* rec = mrg + wave * 4096 + g * 132;
#pragma unroll
      for (int k = 0; k < 8; k += 4) *reinterpret_cast<f32x4*>(rec + 4 + 8 * c + k) = f32x4{acc[g * 8 + k], acc[g * 8 + k + 1], acc[g * 8 + k + 2], acc[g * 8 + k + 3]};
      if (c == g) { rec[0] = m; rec[1] = l; } } }
  drain();
  bar();
  {
    const int g = tid >> 7, d = tid & 127;
    float M = -INFINITY;
#pragma unroll
    for (int w = 0; w < NW; ++w) M = fmaxf(M, mrg[w * 4096 + g * 132]);
    const float Ms = (M == -INFINITY) ? 0.f : M;
    float L = 0.f, o = 0.f;
#pragma unroll
    for (int w = 0; w < NW; ++w) { const float* rec = mrg + w * 4096 + g * 132; const float wt = __builtin_amdgcn_exp2f(rec[0] - Ms);
      L = fmaf(wt, rec[1], L); o = fmaf(wt, rec[4 + d], o); }
    stg(po + (size_t)wg * 512 + tid, tag, __float_as_uint(o));
    if (d == 0) { stg(gml + (wg * 4 + g) * 2, tag, __float_as_uint(M)); stg(gml + (wg * 4 + g) * 2 + 1, tag, __float_as_uint(L)); }
  }
  ST(6);
  const unsigned short* wt = reinterpret_cast<const unsigned short*>(pgl);
  unsigned short* act = reinterpret_cast<unsigned short*>(lds + L_ACT);
  float* part = lds + L_RED;
  const int fn = lane & 15, fk = lane >> 4;
  {
    { const int bb = tid >> 8, hh = (tid >> 7) & 1, d = tid & 127, h = tkg * 2 + hh;
      const int src0 = (bb * 8 + (h >> 2)) * 16;
      unsigned v[16], ml[2];
      sweep<2>(gml + ((src0 + c) * 4 + (h & 3)) * 2, 1, tag, ml);
      prepoll(po + ((size_t)src0 * 4 + (h & 3)) * 128 + d + (size_t)(tid & 15) * 512, tag);
      sweep<16>(po + ((size_t)src0 * 4 + (h & 3)) * 128 + d, 512, tag, v);
      const float ms = __uint_as_float(ml[0]), ls = __uint_as_float(ml[1]);
      float M = ms; M = fmaxf(M, dpp<XOR1>(M)); M = fmaxf(M, dpp<XOR2>(M)); M = fmaxf(M, dpp<XOR7>(M)); M = fmaxf(M, dpp<XOR8>(M));
      const float w = __builtin_amdgcn_exp2f(ms - M);
      float L = (w > 0.f) ? w * ls : 0.f; L += dpp<XOR1>(L); L += dpp<XOR2>(L); L += dpp<XOR7>(L); L += dpp<XOR8>(L);
      const float wn = w / L;
      float o = 0.f;
#pragma unroll
      for (int ss = 0; ss < 16; ++ss) o = fmaf(__builtin_bit_cast(float, __builtin_amdgcn_readlane(__builtin_bit_cast(int, wn), ss)), __uint_as_float(v[ss]), o);
      act[bb * 512 + hh * 128 + d] = (unsigned short)cvtpk(o, o);
    }
    bar();
    ST(7);
    const s16x8 bf = *reinterpret_cast<const s16x8*>(act + (fn & 1) * 512 + wave * 32 + fk * 8);
    const s16x8 a0 = *reinterpret_cast<const s16x8*>(wt + fn * 32 + fk * 8), a1 = *reinterpret_cast<const s16x8*>(wt + 512 + fn * 32 + fk * 8);
    f32x4 d0 = {0.f, 0.f, 0.f, 0.f}, d1 = d0;
    d0 = __builtin_amdgcn_mfma_f32_16x16x32_bf16(a0, bf, d0, 0, 0, 0);
    d1 = __builtin_amdgcn_mfma_f32_16x16x32_bf16(a1, bf, d1, 0, 0, 0);
    if (fn < 2) { *reinterpret_cast<f32x4*>(part + wave * 64 + fn * 32 + fk * 4) = d0; *reinterpret_cast<f32x4*>(part + wave * 64 + fn * 32 + 16 + fk * 4) = d1; }
    bar();
    if (tid < 64) { float sum = 0.f;
#pragma unroll
      for (int w = 0; w < 8; ++w) sum += part[w * 64 + tid];
      stg(yp + (tkg * 2 + (tid >> 5)) * 512 + tcg * 32 + (tid & 31), tag, __float_as_uint(sum)); }
  }
  ST(8);
  if (sw >= 224) return;
  {
    { unsigned v[16], w[16];
      prepoll(yp + tid + (tid & 15) * 1024, tag);
      sweep<16>(yp + tid, 1024, tag, v); sweep<16>(yp + 512 + tid, 1024, tag, w);
      float y0 = 0.f, y1 = 0.f;
#pragma unroll
      for (int k = 0; k < 16; ++k) { y0 += __uint_as_float(v[k]); y1 += __uint_as_float(w[k]); }
      lds[L_XS + tid] = rbf(lds[L_XS + tid] + rbf(modv(lds, 0, 1024 + tid) * rbf(y0)));
      lds[L_XS + 512 + tid] = rbf(lds[L_XS + 512 + tid] + rbf(modv(lds, 1, 1024 + tid) * rbf(y1)));
    }
    bar();
    ST(9);
    ln_mod_all(lds, 1536, 2048, tid);
    const s16x8 b0 = *reinterpret_cast<const s16x8*>(act + (fn & 1) * 512 + wave * 64 + fk * 8), b1 = *reinterpret_cast<const s16x8*>(act + (fn & 1) * 512 + wave * 64 + 32 + fk * 8);
    const s16x8 a0 = *reinterpret_cast<const s16x8*>(wt + 1024 + fn * 64 + fk * 8), a1 = *reinterpret_cast<const s16x8*>(wt + 1024 + fn * 64 + 32 + fk * 8);
    f32x4 d0 = {0.f, 0.f, 0.f, 0.f};
    d0 = __builtin_amdgcn_mfma_f32_16x16x32_bf16(a0, b0, d0, 0, 0, 0);
    d0 = __builtin_amdgcn_mfma_f32_16x16x32_bf16(a1, b1, d0, 0, 0, 0);
    if (fn < 2) *reinterpret_cast<f32x4*>(part + wave * 64 + fn * 32 + fk * 4) = d0;
    bar();
    if (tid < 8) { const int bb = tid >> 2, pr = tid & 3; float g0 = 0.f, g1 = 0.f, u0 = 0.f, u1 = 0.f;
#pragma unroll
      for (int w = 0; w < 8; ++w) { const float* q = part + w * 64 + bb * 32; g0 += q[2 * pr]; g1 += q[2 * pr + 1]; u0 += q[8 + 2 * pr]; u1 += q[8 + 2 * pr + 1]; }
      stg(ghh + bb * 896 + sw * 4 + pr, tag, cvtpk(rbf(g0) * rbf(u0), rbf(g1) * rbf(u1))); }
  }
  ST(10);
  {
    if (tid < 128) { const int bb = tid >> 6, pi = tid & 63; unsigned v[1]; sweep<1>(ghh + bb * 896 + tkg * 64 + pi, 1, tag, v);
      reinterpret_cast<unsigned*>(act)[bb * 256 + pi] = v[0]; }
    bar();
    const s16x8 b0 = *reinterpret_cast<const s16x8*>(act + (fn & 1) * 512 + (wave >> 1) * 32 + fk * 8);
    const s16x8 a0 = *reinterpret_cast<const s16x8*>(wt + 2048 + fn * 32 + fk * 8);
    f32x4 d0 = {0.f, 0.f, 0.f, 0.f};
    d0 = __builtin_amdgcn_mfma_f32_16x16x32_bf16(a0, b0, d0, 0, 0, 0);
    if (fn < 2) *reinterpret_cast<f32x4*>(part + wave * 64 + fn * 32 + fk * 4) = d0;
    bar();
    if (tid < 64) { const int bb = tid >> 5, n = tid & 31, mt = n >> 4; float sum = 0.f;
#pragma unroll
      for (int ks = 0; ks < 4; ++ks) sum += part[(ks * 2 + mt) * 64 + bb * 32 + (n & 15)];
      stg(gop + (tkg * 2 + bb) * 512 + tcg * 32 + n, tag, __float_as_uint(sum)); }
  }
  ST(11);
  if (tkg == 0 && tid < 64) {
    const int bb = tid >> 5, cc = tid & 31, n = tcg * 32 + cc;
    unsigned v[14]; sweep<14>(gop + bb * 512 + n, 1024, tag, v);
    float y = 0.f;
#pragma unroll
    for (int k = 0; k < 14; ++k) y += __uint_as_float(v[k]);
    const float o = lds[L_XS + bb * 512 + n] + rbf(modv(lds, bb, 2560 + n) * rbf(y));
    p.out[bb * 512 + n] = (unsigned short)cvtpk(o, 0.f);
    if (sw == 0 && tid == 0) stw((unsigned*)(ws + WS_EPOCH), tag);
    ST(12);
  }
}

extern "C" void kernel_launch(void* const* d_in, const int* in_sizes, int n_in,
                              void* d_out, int out_size, void* d_ws, size_t ws_size,
                              hipStream_t stream) {
  Params p;
  p.x = (const bf16*)d_in[0]; p.kc = (const bf16*)d_in[1]; p.vc = (const bf16*)d_in[2];
  p.wq = (const bf16*)d_in[3]; p.wo = (const bf16*)d_in[4];
  p.wg = (const bf16*)d_in[6]; p.wu = (const bf16*)d_in[7]; p.wd = (const bf16*)d_in[8];
  p.c = (const bf16*)d_in[9]; p.wmod = (const bf16*)d_in[10];
  p.out = (unsigned short*)d_out; p.ws = (char*)d_ws;
  mega<<<NWG, NT, 0, stream>>>(p);
}
```

```cpp
#include <hip/hip_runtime.h>
#include <hip/hip_bf16.h>
#include <cstdint>

using bf16 = __hip_bfloat16;
using u64 = unsigned long long;
using u32x4  = __attribute__((ext_vector_type(4))) unsigned;
using f32x4  = __attribute__((ext_vector_type(4))) float;
using bf16x2 = __attribute__((ext_vector_type(2))) __bf16;
using s16x8 = __attribute__((ext_vector_type(8))) short;

constexpr int HQ = 32, HKV = 8, SK = 32768, DH = 128, FF = 1792;
constexpr int NSPLIT = 16, NWG = 256, NT = 512, NW = 8;
constexpr int PAGE = 16, NPAGE = SK / PAGE;
constexpr int NSTAT = 14 * NW * 4;
constexpr float SCALE = 0.088388347648318440f;
constexpr unsigned SPIN_CAP = 1u << 22;

constexpr size_t WS_EPOCH = 0;
constexpr size_t WS_CNT  = 1024;
constexpr size_t WS_GMOD = 4096;
constexpr size_t WS_GQ   = WS_GMOD + 2 * 1536 * 8;
constexpr size_t WS_GML  = WS_GQ + 2 * 2048 * 8;
constexpr size_t WS_PO   = WS_GML + 256 * 8 * 8;
constexpr size_t WS_YP   = WS_PO + 256 * 512 * 8;
constexpr size_t WS_GHH  = WS_YP + 16 * 1024 * 8;
constexpr size_t WS_GOP  = WS_GHH + 2 * 896 * 8;
constexpr size_t WS_GQP  = WS_GOP + 14 * 1024 * 8;
constexpr size_t WS_GMP  = WS_GQP + 16 * 8192 * 8;
constexpr size_t WS_GX2  = WS_GMP + 8 * 2048 * 8;

#define ST(k) do {} while (0)
struct Params {
  const bf16 *x, *kc, *vc, *wq, *wo, *wg, *wu, *wd, *c, *wmod;
  unsigned short* out; char* ws;
};

__device__ __forceinline__ float lo2f(unsigned u) { return __builtin_bit_cast(float, u << 16); }
__device__ __forceinline__ float hi2f(unsigned u) { return __builtin_bit_cast(float, u & 0xffff0000u); }
__device__ __forceinline__ unsigned cvtpk(float lo, float hi) {
  unsigned r; asm volatile("v_cvt_pk_bf16_f32 %0, %1, %2" : "=v"(r) : "v"(lo), "v"(hi)); return r;
}
__device__ __forceinline__ float rbf(float x) { return lo2f(cvtpk(x, x)); }
__device__ __forceinline__ float dot2(unsigned k, unsigned q, float acc) {
  return __builtin_amdgcn_fdot2_f32_bf16(__builtin_bit_cast(bf16x2, k), __builtin_bit_cast(bf16x2, q), acc, false);
}
template <int CTRL> __device__ __forceinline__ float dpp(float x) {
  return __builtin_bit_cast(float, __builtin_amdgcn_mov_dpp(__builtin_bit_cast(int, x), CTRL, 0xf, 0xf, true));
}
constexpr int XOR1 = 0xB1, XOR2 = 0x4E, XOR7 = 0x141, XOR8 = 0x128, ROR4 = 0x124;
__device__ __forceinline__ float xrow16_max(float x) {
  auto s = __builtin_amdgcn_permlane16_swap(__float_as_uint(x), __float_as_uint(x), false, false);
  x = fmaxf(__uint_as_float(s[0]), __uint_as_float(s[1]));
  auto t = __builtin_amdgcn_permlane32_swap(__float_as_uint(x), __float_as_uint(x), false, false);
  return fmaxf(__uint_as_float(t[0]), __uint_as_float(t[1]));
}
__device__ __forceinline__ float xrow16_sum(float x) {
  auto s = __builtin_amdgcn_permlane16_swap(__float_as_uint(x), __float_as_uint(x), false, false);
  x = __uint_as_float(s[0]) + __uint_as_float(s[1]);
  auto t = __builtin_amdgcn_permlane32_swap(__float_as_uint(x), __float_as_uint(x), false, false);
  return __uint_as_float(t[0]) + __uint_as_float(t[1]);
}
__device__ __forceinline__ float wave_sum(float x) {
  x += dpp<XOR1>(x); x += dpp<XOR2>(x); x += dpp<XOR7>(x); x += dpp<XOR8>(x); return xrow16_sum(x);
}
__device__ __forceinline__ void bar() { asm volatile("s_waitcnt lgkmcnt(0)\n\ts_barrier" ::: "memory"); }
__device__ __forceinline__ void drain() { asm volatile("s_waitcnt vmcnt(0)" ::: "memory"); }
__device__ __forceinline__ u64 ldg(const u64* p) { return __hip_atomic_load(p, __ATOMIC_RELAXED, __HIP_MEMORY_SCOPE_AGENT); }
__device__ __forceinline__ void stg(u64* p, unsigned tag, unsigned v) { __hip_atomic_store(p, ((u64)tag << 32) | v, __ATOMIC_RELAXED, __HIP_MEMORY_SCOPE_AGENT); }
__device__ __forceinline__ unsigned ldw(const unsigned* p) { return __hip_atomic_load(p, __ATOMIC_RELAXED, __HIP_MEMORY_SCOPE_AGENT); }
__device__ __forceinline__ void stw(unsigned* p, unsigned v) { __hip_atomic_store(p, v, __ATOMIC_RELAXED, __HIP_MEMORY_SCOPE_AGENT); }
template <int N> __device__ __forceinline__ void sweep(const u64* g, int stride, unsigned tag, unsigned (&v)[N]) {
  for (unsigned sp = 0; sp < SPIN_CAP; ++sp) {
    u64 x[N];
#pragma unroll
    for (int k = 0; k < N; ++k) x[k] = ldg(g + (size_t)k * stride);
    bool ok = true;
#pragma unroll
    for (int k = 0; k < N; ++k) { ok = ok && ((unsigned)(x[k] >> 32) == tag); v[k] = (unsigned)x[k]; }
    if (ok) break;
    __builtin_amdgcn_s_sleep(1);
  }
}
__device__ __forceinline__ void prepoll(const u64* g, unsigned tag) {
  for (unsigned sp = 0; sp < SPIN_CAP; ++sp) { if ((unsigned)(ldg(g) >> 32) == tag) break; __builtin_amdgcn_s_sleep(1); }
}
__device__ __forceinline__ void fma8(float (&acc)[16], u32x4 w, float a0, float a1) {
#pragma unroll
  for (int j = 0; j < 4; ++j) {
    const float l = lo2f(w[j]), h = hi2f(w[j]);
    acc[2 * j] = fmaf(a0, l, acc[2 * j]); acc[2 * j + 1] = fmaf(a0, h, acc[2 * j + 1]);
    acc[8 + 2 * j] = fmaf(a1, l, acc[8 + 2 * j]); acc[8 + 2 * j + 1] = fmaf(a1, h, acc[8 + 2 * j + 1]);
  }
}

constexpr int L_PG = 0;
constexpr int L_A = 0;
constexpr int L_XS = L_A + 1024;
constexpr int L_MOD = L_XS + 1024;
constexpr int L_P = L_MOD + 3072;
constexpr int L_Q = L_P + 512;
constexpr int L_RED = L_Q + 256;
constexpr int L_RES = L_RED + 1024;
constexpr int L_ACT = L_RES + 128;
constexpr int L_MODT = L_ACT + 512;
constexpr int L_ST = L_MODT + 512;
constexpr int L_END = L_ST;
#define GLDS(src, dst) __builtin_amdgcn_global_load_lds((const unsigned*)(src), (unsigned*)(dst), 16, 0, 2)
__device__ __forceinline__ float modv(const float* lds, int b, int col) {
  const unsigned v = reinterpret_cast<const unsigned*>(lds)[L_MOD + b * 1536 + (col >> 1)]; return (col & 1) ? hi2f(v) : lo2f(v); }

template <int NCH>
__device__ __forceinline__ void reduce_tile(float (&acc)[16], float* lds, int tid) {
  const int lane = tid & 63, wave = tid >> 6;
#pragma unroll
  for (int j = 0; j < 16; ++j) {
    float v = acc[j];
    if (NCH == 1) { v += dpp<XOR1>(v); v += dpp<XOR2>(v); }
    if (NCH == 2) { v += dpp<XOR2>(v); }
    if (NCH <= 4) { v += dpp<XOR8>(v); v += dpp<ROR4>(v); }
    if (NCH == 8) { v += dpp<XOR8>(v); }
    acc[j] = xrow16_sum(v);
  }
  float* part = lds + L_RED;
  if (lane < NCH) {
#pragma unroll
    for (int j = 0; j < 16; j += 4) *reinterpret_cast<f32x4*>(part + (wave * NCH + lane) * 16 + j) = f32x4{acc[j], acc[j + 1], acc[j + 2], acc[j + 3]};
  }
  bar();
  if (tid < NCH * 16) { float s = 0.f;
#pragma unroll
    for (int w = 0; w < 8; ++w) s += part[w * NCH * 16 + tid];
    lds[L_RES + tid] = s; }
  bar();
}

__device__ __forceinline__ void ln_mod(float* lds, int soff, int coff, int tid) {
  const int lane = tid & 63, wave = tid >> 6;
  if (wave < 2) {
    const float* x = lds + L_XS + wave * 512;
    float xv[8]; float s = 0.f;
#pragma unroll
    for (int i = 0; i < 8; ++i) { xv[i] = x[lane + 64 * i]; s += xv[i]; }
    s = wave_sum(s) * (1.f / 512.f);
    const float m = rbf(s);
    float v = 0.f;
#pragma unroll
    for (int i = 0; i < 8; ++i) { const float d = xv[i] - s; v += d * d; }
    v = rbf(wave_sum(v) * (1.f / 512.f));
    const float den = rbf(sqrtf(rbf(v + 1e-5f)));
#pragma unroll
    for (int i = 0; i < 8; ++i) {
      const float n = rbf(rbf(xv[i] - m) / den);
      lds[L_A + wave * 512 + lane + 64 * i] = rbf(rbf(n * rbf(1.f + modv(lds, wave, coff + lane + 64 * i))) + modv(lds, wave, soff + lane + 64 * i));
    }
  }
  bar();
}
__device__ __forceinline__ void ln_mod_all(float* lds, int soff, int coff, int tid) {
  const int lane = tid & 63;
  float s0 = 0.f, s1 = 0.f, x0[8], x1[8];
#pragma unroll
  for (int i = 0; i < 8; ++i) { x0[i] = lds[L_XS + lane + 64 * i]; x1[i] = lds[L_XS + 512 + lane + 64 * i]; s0 += x0[i]; s1 += x1[i]; }
  s0 = wave_sum(s0) * (1.f / 512.f); s1 = wave_sum(s1) * (1.f / 512.f);
  float v0 = 0.f, v1 = 0.f;
#pragma unroll
  for (int i = 0; i < 8; ++i) { const float d0 = x0[i] - s0, d1 = x1[i] - s1; v0 = fmaf(d0, d0, v0); v1 = fmaf(d1, d1, v1); }
  v0 = rbf(wave_sum(v0) * (1.f / 512.f)); v1 = rbf(wave_sum(v1) * (1.f / 512.f));
  const float den0 = rbf(sqrtf(rbf(v0 + 1e-5f))), den1 = rbf(sqrtf(rbf(v1 + 1e-5f)));
  const float n0 = rbf(rbf(lds[L_XS + tid] - rbf(s0)) / den0), n1 = rbf(rbf(lds[L_XS + 512 + tid] - rbf(s1)) / den1);
  const float h0 = rbf(n0 * rbf(1.f + modv(lds, 0, coff + tid))) + modv(lds, 0, soff + tid);
  const float h1 = rbf(n1 * rbf(1.f + modv(lds, 1, coff + tid))) + modv(lds, 1, soff + tid);
  unsigned short* act = reinterpret_cast<unsigned short*>(lds + L_ACT);
  const unsigned pk = cvtpk(h0, h1);
  act[tid] = (unsigned short)pk; act[512 + tid] = (unsigned short)(pk >> 16);
  bar();
}

__global__ void __launch_bounds__(NT, 2) mega(Params p) {
  __shared__ __attribute__((aligned(16))) float lds[L_END];
  __shared__ __attribute__((aligned(16))) float ldsP[32768];
  const int wg = blockIdx.x, tid = threadIdx.x, lane = tid & 63, wave = __builtin_amdgcn_readfirstlane(tid >> 6);
  const int sw = (wg & 7) * 32 + (wg >> 3);
  const int r = lane >> 4, c = lane & 15;
  const int pair = wg >> 4, s = wg & 15, b = pair >> 3, kvh = pair & 7;
  char* ws = p.ws;
  u64* gmod = (u64*)(ws + WS_GMOD); u64* gq = (u64*)(ws + WS_GQ); u64* gml = (u64*)(ws + WS_GML);
  u64* po = (u64*)(ws + WS_PO); u64* yp = (u64*)(ws + WS_YP);
  u64* ghh = (u64*)(ws + WS_GHH); u64* gop = (u64*)(ws + WS_GOP);
  ST(0);
  const int tkg = sw >> 4, tcg = sw & 15;
  const size_t kvbase = (size_t)(b * HKV + kvh) * SK * DH + (size_t)r * DH + 8 * c;
  float* pgl = ldsP + wave * 4096;
#define ISSUE_LDS(slot, pg_) do { const bf16* kb_ = p.kc + kvbase + (size_t)(pg_) * PAGE * DH; const bf16* vb_ = p.vc + kvbase + (size_t)(pg_) * PAGE * DH; \
    _Pragma("unroll") for (int t_ = 0; t_ < 4; ++t_) { GLDS(kb_ + (size_t)t_ * 4 * DH, pgl + (slot) * 2048 + t_ * 256); GLDS(vb_ + (size_t)t_ * 4 * DH, pgl + (slot) * 2048 + 1024 + t_ * 256); } } while (0)
#define ISSUE(KB, VB, pg_) do { const bf16* kb_ = p.kc + kvbase + (size_t)(pg_) * PAGE * DH; const bf16* vb_ = p.vc + kvbase + (size_t)(pg_) * PAGE * DH; \
    _Pragma("unroll") for (int t_ = 0; t_ < 4; ++t_) { KB[t_] = __builtin_nontemporal_load(reinterpret_cast<const u32x4*>(kb_ + (size_t)t_ * 4 * DH)); VB[t_] = __builtin_nontemporal_load(reinterpret_cast<const u32x4*>(vb_ + (size_t)t_ * 4 * DH)); } } while (0)
  u32x4 K0[4], V0[4], K1[4], V1[4];
  const int mkg = sw >> 6, mcg = sw & 63;
  const int qkg = sw >> 6, qcg = sw & 63;
  const unsigned xpk = reinterpret_cast<const unsigned*>(p.x)[tid];
  const unsigned cv = reinterpret_cast<const unsigned short*>(p.c)[((tid >> 7) & 1) * 512 + mkg * 128 + (tid & 127)];
  const unsigned tag = ldw((const unsigned*)(ws + WS_EPOCH)) + 1u;
  u32x4 wm = u32x4{0, 0, 0, 0};
  if (tid < 256) wm = *reinterpret_cast<const u32x4*>(p.wmod + (size_t)(mkg * 128 + (tid >> 1)) * 3072 + mcg * 16 + (tid & 1) * 8);
  const u32x4 wq0 = *reinterpret_cast<const u32x4*>(p.wq + (size_t)(qkg * 128 + lane) * 4096 + qcg * 64 + wave * 8);
  const u32x4 wq1 = *reinterpret_cast<const u32x4*>(p.wq + (size_t)(qkg * 128 + 64 + lane) * 4096 + qcg * 64 + wave * 8);
  const int pj = s * 128 + wave;
  ISSUE(K0, V0, pj);
  lds[L_XS + 2 * tid] = lo2f(xpk); lds[L_XS + 2 * tid + 1] = hi2f(xpk);
  if (tid < 256) { const float c0 = lo2f(cv); lds[L_A + tid] = rbf(c0 / (1.f + __expf(-c0))); }
  bar();
  float mean0, mean1, den0, den1;
  { float s0 = 0.f, s1 = 0.f, x0[8], x1[8];
#pragma unroll
    for (int i = 0; i < 8; ++i) { x0[i] = lds[L_XS + lane + 64 * i]; x1[i] = lds[L_XS + 512 + lane + 64 * i]; s0 += x0[i]; s1 += x1[i]; }
    s0 = wave_sum(s0) * (1.f / 512.f); s1 = wave_sum(s1) * (1.f / 512.f);
    float v0 = 0.f, v1 = 0.f;
#pragma unroll
    for (int i = 0; i < 8; ++i) { const float d0 = x0[i] - s0, d1 = x1[i] - s1; v0 = fmaf(d0, d0, v0); v1 = fmaf(d1, d1, v1); }
    v0 = rbf(wave_sum(v0) * (1.f / 512.f)); v1 = rbf(wave_sum(v1) * (1.f / 512.f));
    mean0 = rbf(s0); mean1 = rbf(s1); den0 = rbf(sqrtf(rbf(v0 + 1e-5f))); den1 = rbf(sqrtf(rbf(v1 + 1e-5f))); }
  {
    float acc[16];
#pragma unroll
    for (int jj = 0; jj < 16; ++jj) acc[jj] = 0.f;
    if (tid < 256) fma8(acc, wm, lds[L_A + (tid >> 1)], lds[L_A + 128 + (tid >> 1)]);
    reduce_tile<2>(acc, lds, tid);
    if (tid < 32) { const int ch = tid >> 4, bb = (tid >> 3) & 1, j = tid & 7;
      stg((u64*)(ws + WS_GMP) + (mkg * 2 + bb) * 1024 + mcg * 16 + ch * 8 + j, tag, __float_as_uint(lds[L_RES + tid])); }
  }
  ST(1);
  drain(); ISSUE(K1, V1, pj + 8);
  { const int bb = tid >> 8, wh = (tid >> 7) & 1, kk = tid & 127; unsigned v[4];
    sweep<4>((const u64*)(ws + WS_GMP) + bb * 1024 + wh * 512 + qkg * 128 + kk, 2048, tag, v);
    lds[L_MODT + tid] = rbf((__uint_as_float(v[0]) + __uint_as_float(v[1])) + (__uint_as_float(v[2]) + __uint_as_float(v[3]))); }
  bar();
  ST(2);
  if (tid < 256) { const int bb = tid >> 7, kk = tid & 127;
    const float sa = lds[L_MODT + bb * 256 + kk], ca = lds[L_MODT + bb * 256 + 128 + kk];
    const float xx = lds[L_XS + bb * 512 + qkg * 128 + kk];
    const float n = rbf(rbf(xx - (bb ? mean1 : mean0)) / (bb ? den1 : den0));
    lds[L_A + 256 + tid] = rbf(rbf(n * rbf(1.f + ca)) + sa); }
  bar();
  {
    float acc[16];
#pragma unroll
    for (int jj = 0; jj < 16; ++jj) acc[jj] = 0.f;
    fma8(acc, wq0, lds[L_A + 256 + lane], lds[L_A + 256 + 128 + lane]);
    fma8(acc, wq1, lds[L_A + 256 + 64 + lane], lds[L_A + 256 + 128 + 64 + lane]);
    float val = 0.f;
#pragma unroll
    for (int jj = 0; jj < 16; ++jj) { const float v = wave_sum(acc[jj]); val = (lane == jj) ? v : val; }
    if (lane < 16) stg((u64*)(ws + WS_GQP) + (qkg * 2 + (lane >> 3)) * 4096 + qcg * 64 + wave * 8 + (lane & 7), tag, __float_as_uint(val));
  }
  ST(3);
  drain(); ISSUE_LDS(0, pj + 16);
  { unsigned v[4];
    sweep<4>((const u64*)(ws + WS_GQP) + b * 4096 + kvh * 512 + tid, 8192, tag, v);
    const float q = (__uint_as_float(v[0]) + __uint_as_float(v[1])) + (__uint_as_float(v[2]) + __uint_as_float(v[3]));
    const float qn = dpp<XOR1>(q);
    if ((tid & 1) == 0) reinterpret_cast<unsigned*>(lds + L_Q)[tid >> 1] = cvtpk(q, qn); }
  drain(); ISSUE_LDS(1, pj + 24);
  bar();
  ST(4);
  u32x4 qpk[4];
#pragma unroll
  for (int g = 0; g < 4; ++g) qpk[g] = *reinterpret_cast<const u32x4*>(lds + L_Q + g * 64 + 4 * c);

  constexpr float C2 = SCALE * 1.4426950408889634f;
  float m = -INFINITY, l = 0.f, acc[32];
#pragma unroll
  for (int k = 0; k < 32; ++k) acc[k] = 0.f;
  float* prow = lds + L_P + (wave * 4 + r) * 16;
  auto proc = [&](const u32x4 (&Kc)[4], const u32x4 (&Vc)[4]) {
    float x[16];
#pragma unroll
    for (int t = 0; t < 4; ++t)
#pragma unroll
      for (int g = 0; g < 4; ++g) { float d = 0.f;
#pragma unroll
        for (int k = 0; k < 4; ++k) d = dot2(Kc[t][k], qpk[g][k], d);
        x[t * 4 + g] = d; }
#define TR_STEP(HALF, CTRL, BIT) _Pragma("unroll") for (int h = 0; h < (HALF); ++h) { \
      const float lo_ = x[h], hi_ = x[h + (HALF)]; const float t1_ = lo_ + dpp<CTRL>(lo_), t2_ = hi_ + dpp<CTRL>(hi_); \
      x[h] = (c & (BIT)) ? t2_ : t1_; }
    TR_STEP(8, XOR8, 8) TR_STEP(4, XOR7, 4) TR_STEP(2, XOR2, 2) TR_STEP(1, XOR1, 1)
#undef TR_STEP
    const float sc = x[0] * C2;
    float pm = sc; pm = fmaxf(pm, dpp<ROR4>(pm)); pm = fmaxf(pm, dpp<XOR8>(pm)); pm = xrow16_max(pm);
    const float mn = fmaxf(m, pm);
    const float alpha = __builtin_amdgcn_exp2f(m - mn), pp = __builtin_amdgcn_exp2f(sc - mn);
    l = fmaf(l, alpha, pp); m = mn;
#pragma unroll
    for (int q4 = 0; q4 < 4; ++q4) {
      const float al = q4 == 0 ? dpp<0x00>(alpha) : q4 == 1 ? dpp<0x55>(alpha) : q4 == 2 ? dpp<0xAA>(alpha) : dpp<0xFF>(alpha);
#pragma unroll
      for (int k = 0; k < 8; ++k) acc[q4 * 8 + k] *= al; }
    prow[c] = pp;
    asm volatile("s_waitcnt lgkmcnt(0)" ::: "memory");
    f32x4 pr[4];
#pragma unroll
    for (int u = 0; u < 4; ++u) pr[u] = *reinterpret_cast<const f32x4*>(prow + 4 * u);
#pragma unroll
    for (int t = 0; t < 4; ++t) {
      float vf[8];
#pragma unroll
      for (int k = 0; k < 4; ++k) { vf[2 * k] = lo2f(Vc[t][k]); vf[2 * k + 1] = hi2f(Vc[t][k]); }
#pragma unroll
      for (int g = 0; g < 4; ++g) { const float pg_ = pr[t][g];
#pragma unroll
        for (int k = 0; k < 8; ++k) acc[g * 8 + k] = fmaf(pg_, vf[k], acc[g * 8 + k]); } }
  };
  proc(K0, V0); __builtin_amdgcn_sched_barrier(0); ISSUE(K0, V0, pj + 4 * 8); __builtin_amdgcn_sched_barrier(0);
  proc(K1, V1); __builtin_amdgcn_sched_barrier(0);
  {
    asm volatile("s_waitcnt vmcnt(8)" ::: "memory");
    ISSUE(K1, V1, pj + 5 * 8);
#pragma unroll 1
    for (int i = 0; i < 2; ++i) {
      u32x4 Kt[4], Vt[4];
#pragma unroll
      for (int t = 0; t < 4; ++t) { Kt[t] = *reinterpret_cast<const u32x4*>(pgl + i * 2048 + t * 256 + lane * 4); Vt[t] = *reinterpret_cast<const u32x4*>(pgl + i * 2048 + 1024 + t * 256 + lane * 4); }
      proc(Kt, Vt);
    }
  }
  const u32x4 wm2 = *reinterpret_cast<const u32x4*>(p.wmod + (size_t)tid * 3072 + 1024 + sw * 8);
  const unsigned cpk2a = reinterpret_cast<const unsigned short*>(p.c)[tid], cpk2b = reinterpret_cast<const unsigned short*>(p.c)[512 + tid];
  { float acc[16];
#pragma unroll
    for (int jj = 0; jj < 16; ++jj) acc[jj] = 0.f;
    const float c0 = lo2f(cpk2a), c1 = lo2f(cpk2b);
    fma8(acc, wm2, rbf(c0 / (1.f + __expf(-c0))), rbf(c1 / (1.f + __expf(-c1))));
    reduce_tile<1>(acc, lds, tid);
    if (tid < 8) { const int bb = tid >> 2, pr = tid & 3;
      stg(gmod + bb * 1536 + 512 + sw * 4 + pr, tag, cvtpk(lds[L_RES + bb * 8 + 2 * pr], lds[L_RES + bb * 8 + 2 * pr + 1])); } }
  {
    asm volatile("s_waitcnt lgkmcnt(0)" ::: "memory");
    unsigned short* wt = reinterpret_cast<unsigned short*>(pgl);
    u32x4 t0 = *reinterpret_cast<const u32x4*>(p.wo + (size_t)(tkg * 256 + wave * 32 + (lane >> 2)) * 512 + tcg * 32 + (lane & 3) * 8);
    u32x4 t1 = *reinterpret_cast<const u32x4*>(p.wo + (size_t)(tkg * 256 + wave * 32 + 16 + (lane >> 2)) * 512 + tcg * 32 + (lane & 3) * 8);
    u32x4 t2 = u32x4{0, 0, 0, 0}, t3 = t2, t4 = t2;
    if (sw < 224) {
      t2 = *reinterpret_cast<const u32x4*>(p.wg + (size_t)(wave * 64 + lane) * FF + sw * 8);
      t3 = *reinterpret_cast<const u32x4*>(p.wu + (size_t)(wave * 64 + lane) * FF + sw * 8);
      t4 = *reinterpret_cast<const u32x4*>(p.wd + (size_t)(tkg * 128 + (wave >> 1) * 32 + (lane >> 1)) * 512 + tcg * 32 + (wave & 1) * 16 + (lane & 1) * 8);
    }
#pragma unroll
    for (int e = 0; e < 8; ++e) {
      const int sh = (e & 1) * 16, ix = e >> 1;
      const int n = (lane & 3) * 8 + e;
      wt[(n >> 4) * 512 + (n & 15) * 32 + (lane >> 2)] = (unsigned short)(t0[ix] >> sh);
      wt[(n >> 4) * 512 + (n & 15) * 32 + 16 + (lane >> 2)] = (unsigned short)(t1[ix] >> sh);
      wt[1024 + e * 64 + lane] = (unsigned short)(t2[ix] >> sh);
      wt[1024 + (8 + e) * 64 + lane] = (unsigned short)(t3[ix] >> sh);
      wt[2048 + ((lane & 1) * 8 + e) * 32 + (lane >> 1)] = (unsigned short)(t4[ix] >> sh);
    }
  }
#pragma unroll 1
  for (int i = 4; i < 16; i += 2) {
    proc(K0, V0); __builtin_amdgcn_sched_barrier(0);
    if (i + 2 < 16) ISSUE(K0, V0, pj + (i + 2) * 8);
    __builtin_amdgcn_sched_barrier(0);
    proc(K1, V1); __builtin_amdgcn_sched_barrier(0);
    if (i + 3 < 16) ISSUE(K1, V1, pj + (i + 3) * 8);
    __builtin_amdgcn_sched_barrier(0);
  }
#undef ISSUE
#undef ISSUE_LDS
  ST(5);
  { unsigned v[2], w[2];
    sweep<2>(gmod + 512 + tid, 512, tag, v); sweep<2>(gmod + 1536 + 512 + tid, 512, tag, w);
#pragma unroll
    for (int i = 0; i < 2; ++i) { const int pi = 512 + 512 * i + tid;
      reinterpret_cast<unsigned*>(lds)[L_MOD + pi] = v[i]; reinterpret_cast<unsigned*>(lds)[L_MOD + 1536 + pi] = w[i]; } }
#pragma unroll
  for (int k = 0; k < 32; ++k) acc[k] = xrow16_sum(acc[k]);
  { float t = l; t += dpp<ROR4>(t); t += dpp<XOR8>(t); l = xrow16_sum(t); }
  float* mrg = ldsP + 2048;
  if (r == 0) {
#pragma unroll
    for (int g = 0; g < 4; ++g) {
      float* rec = mrg + wave * 4096 + g * 132;
#pragma unroll
      for (int k = 0; k < 8; k += 4) *reinterpret_cast<f32x4*>(rec + 4 + 8 * c + k) = f32x4{acc[g * 8 + k], acc[g * 8 + k + 1], acc[g * 8 + k + 2], acc[g * 8 + k + 3]};
      if (c == g) { rec[0] = m; rec[1] = l; } } }
  drain();
  bar();
  {
    const int g = tid >> 7, d = tid & 127;
    float M = -INFINITY;
#pragma unroll
    for (int w = 0; w < NW; ++w) M = fmaxf(M, mrg[w * 4096 + g * 132]);
    const float Ms = (M == -INFINITY) ? 0.f : M;
    float L = 0.f, o = 0.f;
#pragma unroll
    for (int w = 0; w < NW; ++w) { const float* rec = mrg + w * 4096 + g * 132; const float wt = __builtin_amdgcn_exp2f(rec[0] - Ms);
      L = fmaf(wt, rec[1], L); o = fmaf(wt, rec[4 + d], o); }
    stg(po + (size_t)wg * 512 + tid, tag, __float_as_uint(o));
    if (d == 0) { stg(gml + (wg * 4 + g) * 2, tag, __float_as_uint(M)); stg(gml + (wg * 4 + g) * 2 + 1, tag, __float_as_uint(L)); }
  }
  ST(6);
  const unsigned short* wt = reinterpret_cast<const unsigned short*>(pgl);
  unsigned short* act = reinterpret_cast<unsigned short*>(lds + L_ACT);
  float* part = lds + L_RED;
  const int fn = lane & 15, fk = lane >> 4;
  {
    { const int bb = tid >> 8, hh = (tid >> 7) & 1, d = tid & 127, h = tkg * 2 + hh;
      const int src0 = (bb * 8 + (h >> 2)) * 16;
      unsigned v[16], ml[2];
      sweep<2>(gml + ((src0 + c) * 4 + (h & 3)) * 2, 1, tag, ml);
      prepoll(po + ((size_t)src0 * 4 + (h & 3)) * 128 + d + (size_t)(tid & 15) * 512, tag);
      sweep<16>(po + ((size_t)src0 * 4 + (h & 3)) * 128 + d, 512, tag, v);
      const float ms = __uint_as_float(ml[0]), ls = __uint_as_float(ml[1]);
      float M = ms; M = fmaxf(M, dpp<XOR1>(M)); M = fmaxf(M, dpp<XOR2>(M)); M = fmaxf(M, dpp<XOR7>(M)); M = fmaxf(M, dpp<XOR8>(M));
      const float w = __builtin_amdgcn_exp2f(ms - M);
      float L = (w > 0.f) ? w * ls : 0.f; L += dpp<XOR1>(L); L += dpp<XOR2>(L); L += dpp<XOR7>(L); L += dpp<XOR8>(L);
      const float wn = w / L;
      float o = 0.f;
#pragma unroll
      for (int ss = 0; ss < 16; ++ss) o = fmaf(__builtin_bit_cast(float, __builtin_amdgcn_readlane(__builtin_bit_cast(int, wn), ss)), __uint_as_float(v[ss]), o);
      act[bb * 512 + hh * 128 + d] = (unsigned short)cvtpk(o, o);
    }
    bar();
    ST(7);
    const s16x8 bf = *reinterpret_cast<const s16x8*>(act + (fn & 1) * 512 + wave * 32 + fk * 8);
    const s16x8 a0 = *reinterpret_cast<const s16x8*>(wt + fn * 32 + fk * 8), a1 = *reinterpret_cast<const s16x8*>(wt + 512 + fn * 32 + fk * 8);
    f32x4 d0 = {0.f, 0.f, 0.f, 0.f}, d1 = d0;
    d0 = __builtin_amdgcn_mfma_f32_16x16x32_bf16(a0, bf, d0, 0, 0, 0);
    d1 = __builtin_amdgcn_mfma_f32_16x16x32_bf16(a1, bf, d1, 0, 0, 0);
    if (fn < 2) { *reinterpret_cast<f32x4*>(part + wave * 64 + fn * 32 + fk * 4) = d0; *reinterpret_cast<f32x4*>(part + wave * 64 + fn * 32 + 16 + fk * 4) = d1; }
    bar();
    if (tid < 64) { float sum = 0.f;
#pragma unroll
      for (int w = 0; w < 8; ++w) sum += part[w * 64 + tid];
      stg(yp + (tkg * 2 + (tid >> 5)) * 512 + tcg * 32 + (tid & 31), tag, __float_as_uint(sum)); }
  }
  ST(8);
  if (wave == 0) {
    const int o = sw * 4 + (lane >> 4), k = lane & 15;
    const float xo = lo2f(reinterpret_cast<const unsigned short*>(p.x)[o]);
    unsigned v[1]; sweep<1>(yp + k * 1024 + o, 1, tag, v);
    float y = __uint_as_float(v[0]);
    y += dpp<XOR1>(y); y += dpp<XOR2>(y); y += dpp<XOR7>(y); y += dpp<XOR8>(y);
    const float x2 = rbf(xo + rbf(modv(lds, o >> 9, 1024 + (o & 511)) * rbf(y)));
    const float x2n = __shfl_xor(x2, 16);
    if ((lane & 31) == 0) stg((u64*)(ws + WS_GX2) + (o >> 1), tag, cvtpk(x2, x2n));
  }
  if (sw >= 224) return;
  {
    { unsigned v[1]; sweep<1>((const u64*)(ws + WS_GX2) + tid, 1, tag, v);
      lds[L_XS + 2 * tid] = lo2f(v[0]); lds[L_XS + 2 * tid + 1] = hi2f(v[0]); }
    bar();
    ST(9);
    ln_mod_all(lds, 1536, 2048, tid);
    const s16x8 b0 = *reinterpret_cast<const s16x8*>(act + (fn & 1) * 512 + wave * 64 + fk * 8), b1 = *reinterpret_cast<const s16x8*>(act + (fn & 1) * 512 + wave * 64 + 32 + fk * 8);
    const s16x8 a0 = *reinterpret_cast<const s16x8*>(wt + 1024 + fn * 64 + fk * 8), a1 = *reinterpret_cast<const s16x8*>(wt + 1024 + fn * 64 + 32 + fk * 8);
    f32x4 d0 = {0.f, 0.f, 0.f, 0.f};
    d0 = __builtin_amdgcn_mfma_f32_16x16x32_bf16(a0, b0, d0, 0, 0, 0);
    d0 = __builtin_amdgcn_mfma_f32_16x16x32_bf16(a1, b1, d0, 0, 0, 0);
    if (fn < 2) *reinterpret_cast<f32x4*>(part + wave * 64 + fn * 32 + fk * 4) = d0;
    bar();
    if (tid < 8) { const int bb = tid >> 2, pr = tid & 3; float g0 = 0.f, g1 = 0.f, u0 = 0.f, u1 = 0.f;
#pragma unroll
      for (int w = 0; w < 8; ++w) { const float* q = part + w * 64 + bb * 32; g0 += q[2 * pr]; g1 += q[2 * pr + 1]; u0 += q[8 + 2 * pr]; u1 += q[8 + 2 * pr + 1]; }
      stg(ghh + bb * 896 + sw * 4 + pr, tag, cvtpk(rbf(g0) * rbf(u0), rbf(g1) * rbf(u1))); }
  }
  ST(10);
  {
    if (tid < 128) { const int bb = tid >> 6, pi = tid & 63; unsigned v[1]; sweep<1>(ghh + bb * 896 + tkg * 64 + pi, 1, tag, v);
      reinterpret_cast<unsigned*>(act)[bb * 256 + pi] = v[0]; }
    bar();
    const s16x8 b0 = *reinterpret_cast<const s16x8*>(act + (fn & 1) * 512 + (wave >> 1) * 32 + fk * 8);
    const s16x8 a0 = *reinterpret_cast<const s16x8*>(wt + 2048 + fn * 32 + fk * 8);
    f32x4 d0 = {0.f, 0.f, 0.f, 0.f};
    d0 = __builtin_amdgcn_mfma_f32_16x16x32_bf16(a0, b0, d0, 0, 0, 0);
    if (fn < 2) *reinterpret_cast<f32x4*>(part + wave * 64 + fn * 32 + fk * 4) = d0;
    bar();
    if (tid < 64) { const int bb = tid >> 5, n = tid & 31, mt = n >> 4; float sum = 0.f;
#pragma unroll
      for (int ks = 0; ks < 4; ++ks) sum += part[(ks * 2 + mt) * 64 + bb * 32 + (n & 15)];
      stg(gop + (tkg * 2 + bb) * 512 + tcg * 32 + n, tag, __float_as_uint(sum)); }
  }
  ST(11);
  if (tkg == 0 && tid < 64) {
    const int bb = tid >> 5, cc = tid & 31, n = tcg * 32 + cc;
    unsigned v[14]; sweep<14>(gop + bb * 512 + n, 1024, tag, v);
    float y = 0.f;
#pragma unroll
    for (int k = 0; k < 14; ++k) y += __uint_as_float(v[k]);
    const float o = lds[L_XS + bb * 512 + n] + rbf(modv(lds, bb, 2560 + n) * rbf(y));
    p.out[bb * 512 + n] = (unsigned short)cvtpk(o, 0.f);
    if (sw == 0 && tid == 0) stw((unsigned*)(ws + WS_EPOCH), tag);
    ST(12);
  }
}

extern "C" void kernel_launch(void* const* d_in, const int* in_sizes, int n_in,
                              void* d_out, int out_size, void* d_ws, size_t ws_size,
                              hipStream_t stream) {
  Params p;
  p.x = (const bf16*)d_in[0]; p.kc = (const bf16*)d_in[1]; p.vc = (const bf16*)d_in[2];
  p.wq = (const bf16*)d_in[3]; p.wo = (const bf16*)d_in[4];
  p.wg = (const bf16*)d_in[6]; p.wu = (const bf16*)d_in[7]; p.wd = (const bf16*)d_in[8];
  p.c = (const bf16*)d_in[9]; p.wmod = (const bf16*)d_in[10];
  p.out = (unsigned short*)d_out; p.ws = (char*)d_ws;
  mega<<<NWG, NT, 0, stream>>>(p);
}
```

```cpp
#include <hip/hip_runtime.h>
#include <hip/hip_bf16.h>
#include <cstdint>

using bf16 = __hip_bfloat16;
using u64 = unsigned long long;
using u32x4  = __attribute__((ext_vector_type(4))) unsigned;
using f32x4  = __attribute__((ext_vector_type(4))) float;
using bf16x2 = __attribute__((ext_vector_type(2))) __bf16;
using s16x8 = __attribute__((ext_vector_type(8))) short;

constexpr int HQ = 32, HKV = 8, SK = 32768, DH = 128, FF = 1792;
constexpr int NSPLIT = 16, NWG = 256, NT = 512, NW = 8;
constexpr int PAGE = 16, NPAGE = SK / PAGE;
constexpr int NSTAT = 14 * NW * 4;
constexpr float SCALE = 0.088388347648318440f;
constexpr unsigned SPIN_CAP = 1u << 22;

constexpr size_t WS_EPOCH = 0;
constexpr size_t WS_CNT  = 1024;
constexpr size_t WS_GMOD = 4096;
constexpr size_t WS_GQ   = WS_GMOD + 2 * 1536 * 8;
constexpr size_t WS_GML  = WS_GQ + 2 * 2048 * 8;
constexpr size_t WS_PO   = WS_GML + 256 * 8 * 8;
constexpr size_t WS_YP   = WS_PO + 256 * 512 * 8;
constexpr size_t WS_GHH  = WS_YP + 16 * 1024 * 8;
constexpr size_t WS_GOP  = WS_GHH + 2 * 896 * 8;
constexpr size_t WS_GQP  = WS_GOP + 14 * 1024 * 8;
constexpr size_t WS_GMP  = WS_GQP + 16 * 8192 * 8;
constexpr size_t WS_GX2  = WS_GMP + 8 * 2048 * 8;

#define ST(k) do {} while (0)
struct Params {
  const bf16 *x, *kc, *vc, *wq, *wo, *wg, *wu, *wd, *c, *wmod;
  unsigned short* out; char* ws;
};

__device__ __forceinline__ float lo2f(unsigned u) { return __builtin_bit_cast(float, u << 16); }
__device__ __forceinline__ float hi2f(unsigned u) { return __builtin_bit_cast(float, u & 0xffff0000u); }
__device__ __forceinline__ unsigned cvtpk(float lo, float hi) {
  unsigned r; asm volatile("v_cvt_pk_bf16_f32 %0, %1, %2" : "=v"(r) : "v"(lo), "v"(hi)); return r;
}
__device__ __forceinline__ float rbf(float x) { return lo2f(cvtpk(x, x)); }
__device__ __forceinline__ float dot2(unsigned k, unsigned q, float acc) {
  return __builtin_amdgcn_fdot2_f32_bf16(__builtin_bit_cast(bf16x2, k), __builtin_bit_cast(bf16x2, q), acc, false);
}
template <int CTRL> __device__ __forceinline__ float dpp(float x) {
  return __builtin_bit_cast(float, __builtin_amdgcn_mov_dpp(__builtin_bit_cast(int, x), CTRL, 0xf, 0xf, true));
}
constexpr int XOR1 = 0xB1, XOR2 = 0x4E, XOR7 = 0x141, XOR8 = 0x128, ROR4 = 0x124;
__device__ __forceinline__ float xrow16_max(float x) {
  auto s = __builtin_amdgcn_permlane16_swap(__float_as_uint(x), __float_as_uint(x), false, false);
  x = fmaxf(__uint_as_float(s[0]), __uint_as_float(s[1]));
  auto t = __builtin_amdgcn_permlane32_swap(__float_as_uint(x), __float_as_uint(x), false, false);
  return fmaxf(__uint_as_float(t[0]), __uint_as_float(t[1]));
}
__device__ __forceinline__ float xrow16_sum(float x) {
  auto s = __builtin_amdgcn_permlane16_swap(__float_as_uint(x), __float_as_uint(x), false, false);
  x = __uint_as_float(s[0]) + __uint_as_float(s[1]);
  auto t = __builtin_amdgcn_permlane32_swap(__float_as_uint(x), __float_as_uint(x), false, false);
  return __uint_as_float(t[0]) + __uint_as_float(t[1]);
}
__device__ __forceinline__ float wave_sum(float x) {
  x += dpp<XOR1>(x); x += dpp<XOR2>(x); x += dpp<XOR7>(x); x += dpp<XOR8>(x); return xrow16_sum(x);
}
__device__ __forceinline__ void bar() { asm volatile("s_waitcnt lgkmcnt(0)\n\ts_barrier" ::: "memory"); }
__device__ __forceinline__ void drain() { asm volatile("s_waitcnt vmcnt(0)" ::: "memory"); }
__device__ __forceinline__ u64 ldg(const u64* p) { return __hip_atomic_load(p, __ATOMIC_RELAXED, __HIP_MEMORY_SCOPE_AGENT); }
__device__ __forceinline__ void stg(u64* p, unsigned tag, unsigned v) { __hip_atomic_store(p, ((u64)tag << 32) | v, __ATOMIC_RELAXED, __HIP_MEMORY_SCOPE_AGENT); }
__device__ __forceinline__ unsigned ldw(const unsigned* p) { return __hip_atomic_load(p, __ATOMIC_RELAXED, __HIP_MEMORY_SCOPE_AGENT); }
__device__ __forceinline__ void stw(unsigned* p, unsigned v) { __hip_atomic_store(p, v, __ATOMIC_RELAXED, __HIP_MEMORY_SCOPE_AGENT); }
template <int N> __device__ __forceinline__ void sweep(const u64* g, int stride, unsigned tag, unsigned (&v)[N]) {
  for (unsigned sp = 0; sp < SPIN_CAP; ++sp) {
    u64 x[N];
#pragma unroll
    for (int k = 0; k < N; ++k) x[k] = ldg(g + (size_t)k * stride);
    bool ok = true;
#pragma unroll
    for (int k = 0; k < N; ++k) { ok = ok && ((unsigned)(x[k] >> 32) == tag); v[k] = (unsigned)x[k]; }
    if (ok) break;
    __builtin_amdgcn_s_sleep(1);
  }
}
__device__ __forceinline__ void prepoll(const u64* g, unsigned tag) {
  for (unsigned sp = 0; sp < SPIN_CAP; ++sp) { if ((unsigned)(ldg(g) >> 32) == tag) break; __builtin_amdgcn_s_sleep(1); }
}
__device__ __forceinline__ void fma8(float (&acc)[16], u32x4 w, float a0, float a1) {
#pragma unroll
  for (int j = 0; j < 4; ++j) {
    const float l = lo2f(w[j]), h = hi2f(w[j]);
    acc[2 * j] = fmaf(a0, l, acc[2 * j]); acc[2 * j + 1] = fmaf(a0, h, acc[2 * j + 1]);
    acc[8 + 2 * j] = fmaf(a1, l, acc[8 + 2 * j]); acc[8 + 2 * j + 1] = fmaf(a1, h, acc[8 + 2 * j + 1]);
  }
}

constexpr int L_PG = 0;
constexpr int L_A = 0;
constexpr int L_XS = L_A + 1024;
constexpr int L_MOD = L_XS + 1024;
constexpr int L_P = L_MOD + 3072;
constexpr int L_Q = L_P + 512;
constexpr int L_RED = L_Q + 256;
constexpr int L_RES = L_RED + 1024;
constexpr int L_ACT = L_RES + 128;
constexpr int L_MODT = L_ACT + 512;
constexpr int L_ST = L_MODT + 512;
constexpr int L_END = L_ST;
#define GLDS(src, dst) __builtin_amdgcn_global_load_lds((const unsigned*)(src), (unsigned*)(dst), 16, 0, 2)
__device__ __forceinline__ float modv(const float* lds, int b, int col) {
  const unsigned v = reinterpret_cast<const unsigned*>(lds)[L_MOD + b * 1536 + (col >> 1)]; return (col & 1) ? hi2f(v) : lo2f(v); }

template <int NCH>
__device__ __forceinline__ void reduce_tile(float (&acc)[16], float* lds, int tid) {
  const int lane = tid & 63, wave = tid >> 6;
#pragma unroll
  for (int j = 0; j < 16; ++j) {
    float v = acc[j];
    if (NCH == 1) { v += dpp<XOR1>(v); v += dpp<XOR2>(v); }
    if (NCH == 2) { v += dpp<XOR2>(v); }
    if (NCH <= 4) { v += dpp<XOR8>(v); v += dpp<ROR4>(v); }
    if (NCH == 8) { v += dpp<XOR8>(v); }
    acc[j] = xrow16_sum(v);
  }
  float* part = lds + L_RED;
  if (lane < NCH) {
#pragma unroll
    for (int j = 0; j < 16; j += 4) *reinterpret_cast<f32x4*>(part + (wave * NCH + lane) * 16 + j) = f32x4{acc[j], acc[j + 1], acc[j + 2], acc[j + 3]};
  }
  bar();
  if (tid < NCH * 16) { float s = 0.f;
#pragma unroll
    for (int w = 0; w < 8; ++w) s += part[w * NCH * 16 + tid];
    lds[L_RES + tid] = s; }
  bar();
}

__device__ __forceinline__ void ln_mod_all(float* lds, int soff, int coff, int tid) {
  const int lane = tid & 63;
  float s0 = 0.f, s1 = 0.f, x0[8], x1[8];
#pragma unroll
  for (int i = 0; i < 8; ++i) { x0[i] = lds[L_XS + lane + 64 * i]; x1[i] = lds[L_XS + 512 + lane + 64 * i]; s0 += x0[i]; s1 += x1[i]; }
  s0 = wave_sum(s0) * (1.f / 512.f); s1 = wave_sum(s1) * (1.f / 512.f);
  float v0 = 0.f, v1 = 0.f;
#pragma unroll
  for (int i = 0; i < 8; ++i) { const float d0 = x0[i] - s0, d1 = x1[i] - s1; v0 = fmaf(d0, d0, v0); v1 = fmaf(d1, d1, v1); }
  v0 = rbf(wave_sum(v0) * (1.f / 512.f)); v1 = rbf(wave_sum(v1) * (1.f / 512.f));
  const float den0 = rbf(sqrtf(rbf(v0 + 1e-5f))), den1 = rbf(sqrtf(rbf(v1 + 1e-5f)));
  const float n0 = rbf(rbf(lds[L_XS + tid] - rbf(s0)) / den0), n1 = rbf(rbf(lds[L_XS + 512 + tid] - rbf(s1)) / den1);
  const float h0 = rbf(n0 * rbf(1.f + modv(lds, 0, coff + tid))) + modv(lds, 0, soff + tid);
  const float h1 = rbf(n1 * rbf(1.f + modv(lds, 1, coff + tid))) + modv(lds, 1, soff + tid);
  unsigned short* act = reinterpret_cast<unsigned short*>(lds + L_ACT);
  const unsigned pk = cvtpk(h0, h1);
  act[tid] = (unsigned short)pk; act[512 + tid] = (unsigned short)(pk >> 16);
  bar();
}

__global__ void __launch_bounds__(NT, 2) mega(Params p) {
  __shared__ __attribute__((aligned(16))) float lds[L_END];
  __shared__ __attribute__((aligned(16))) float ldsP[32768];
  const int wg = blockIdx.x, tid = threadIdx.x, lane = tid & 63, wave = __builtin_amdgcn_readfirstlane(tid >> 6);
  const int sw = (wg & 7) * 32 + (wg >> 3);
  const int r = lane >> 4, c = lane & 15;
  const int pair = wg >> 4, s = wg & 15, b = pair >> 3, kvh = pair & 7;
  char* ws = p.ws;
  u64* gmod = (u64*)(ws + WS_GMOD); u64* gq = (u64*)(ws + WS_GQ); u64* gml = (u64*)(ws + WS_GML);
  u64* po = (u64*)(ws + WS_PO); u64* yp = (u64*)(ws + WS_YP);
  u64* ghh = (u64*)(ws + WS_GHH); u64* gop = (u64*)(ws + WS_GOP);
  ST(0);
  const int tkg = sw >> 4, tcg = sw & 15;
  const size_t kvbase = (size_t)(b * HKV + kvh) * SK * DH + (size_t)r * DH + 8 * c;
  float* pgl = ldsP + wave * 4096;
#define ISSUE_LDS(slot, pg_) do { const bf16* kb_ = p.kc + kvbase + (size_t)(pg_) * PAGE * DH; const bf16* vb_ = p.vc + kvbase + (size_t)(pg_) * PAGE * DH; \
    _Pragma("unroll") for (int t_ = 0; t_ < 4; ++t_) { GLDS(kb_ + (size_t)t_ * 4 * DH, pgl + (slot) * 2048 + t_ * 256); GLDS(vb_ + (size_t)t_ * 4 * DH, pgl + (slot) * 2048 + 1024 + t_ * 256); } } while (0)
#define ISSUE(KB, VB, pg_) do { const bf16* kb_ = p.kc + kvbase + (size_t)(pg_) * PAGE * DH; const bf16* vb_ = p.vc + kvbase + (size_t)(pg_) * PAGE * DH; \
    _Pragma("unroll") for (int t_ = 0; t_ < 4; ++t_) { KB[t_] = __builtin_nontemporal_load(reinterpret_cast<const u32x4*>(kb_ + (size_t)t_ * 4 * DH)); VB[t_] = __builtin_nontemporal_load(reinterpret_cast<const u32x4*>(vb_ + (size_t)t_ * 4 * DH)); } } while (0)
  u32x4 K0[4], V0[4], K1[4], V1[4];
  const int mkg = sw >> 6, mcg = sw & 63;
  const int qkg = sw >> 6, qcg = sw & 63;
  const unsigned xpk = reinterpret_cast<const unsigned*>(p.x)[tid];
  const unsigned cv = reinterpret_cast<const unsigned short*>(p.c)[((tid >> 7) & 1) * 512 + mkg * 128 + (tid & 127)];
  const unsigned tag = ldw((const unsigned*)(ws + WS_EPOCH)) + 1u;
  u32x4 wm = u32x4{0, 0, 0, 0};
  if (tid < 256) wm = *reinterpret_cast<const u32x4*>(p.wmod + (size_t)(mkg * 128 + (tid >> 1)) * 3072 + mcg * 16 + (tid & 1) * 8);
  const u32x4 wq0 = *reinterpret_cast<const u32x4*>(p.wq + (size_t)(qkg * 128 + lane) * 4096 + qcg * 64 + wave * 8);
  const u32x4 wq1 = *reinterpret_cast<const u32x4*>(p.wq + (size_t)(qkg * 128 + 64 + lane) * 4096 + qcg * 64 + wave * 8);
  const int pj = s * 128 + wave;
  ISSUE(K0, V0, pj);
  lds[L_XS + 2 * tid] = lo2f(xpk); lds[L_XS + 2 * tid + 1] = hi2f(xpk);
  if (tid < 256) { const float c0 = lo2f(cv); lds[L_A + tid] = rbf(c0 / (1.f + __expf(-c0))); }
  bar();
  float mean0, mean1, den0, den1;
  { float s0 = 0.f, s1 = 0.f, x0[8], x1[8];
#pragma unroll
    for (int i = 0; i < 8; ++i) { x0[i] = lds[L_XS + lane + 64 * i]; x1[i] = lds[L_XS + 512 + lane + 64 * i]; s0 += x0[i]; s1 += x1[i]; }
    s0 = wave_sum(s0) * (1.f / 512.f); s1 = wave_sum(s1) * (1.f / 512.f);
    float v0 = 0.f, v1 = 0.f;
#pragma unroll
    for (int i = 0; i < 8; ++i) { const float d0 = x0[i] - s0, d1 = x1[i] - s1; v0 = fmaf(d0, d0, v0); v1 = fmaf(d1, d1, v1); }
    v0 = rbf(wave_sum(v0) * (1.f / 512.f)); v1 = rbf(wave_sum(v1) * (1.f / 512.f));
    mean0 = rbf(s0); mean1 = rbf(s1); den0 = rbf(sqrtf(rbf(v0 + 1e-5f))); den1 = rbf(sqrtf(rbf(v1 + 1e-5f))); }
  {
    float acc[16];
#pragma unroll
    for (int jj = 0; jj < 16; ++jj) acc[jj] = 0.f;
    if (tid < 256) fma8(acc, wm, lds[L_A + (tid >> 1)], lds[L_A + 128 + (tid >> 1)]);
    reduce_tile<2>(acc, lds, tid);
    if (tid < 32) { const int ch = tid >> 4, bb = (tid >> 3) & 1, j = tid & 7;
      stg((u64*)(ws + WS_GMP) + (mkg * 2 + bb) * 1024 + mcg * 16 + ch * 8 + j, tag, __float_as_uint(lds[L_RES + tid])); }
  }
  ST(1);
  drain(); ISSUE(K1, V1, pj + 8);
  { const int bb = tid >> 8, wh = (tid >> 7) & 1, kk = tid & 127; unsigned v[4];
    sweep<4>((const u64*)(ws + WS_GMP) + bb * 1024 + wh * 512 + qkg * 128 + kk, 2048, tag, v);
    lds[L_MODT + tid] = rbf((__uint_as_float(v[0]) + __uint_as_float(v[1])) + (__uint_as_float(v[2]) + __uint_as_float(v[3]))); }
  bar();
  ST(2);
  if (tid < 256) { const int bb = tid >> 7, kk = tid & 127;
    const float sa = lds[L_MODT + bb * 256 + kk], ca = lds[L_MODT + bb * 256 + 128 + kk];
    const float xx = lds[L_XS + bb * 512 + qkg * 128 + kk];
    const float n = rbf(rbf(xx - (bb ? mean1 : mean0)) / (bb ? den1 : den0));
    lds[L_A + 256 + tid] = rbf(rbf(n * rbf(1.f + ca)) + sa); }
  bar();
  {
    float acc[16];
#pragma unroll
    for (int jj = 0; jj < 16; ++jj) acc[jj] = 0.f;
    fma8(acc, wq0, lds[L_A + 256 + lane], lds[L_A + 256 + 128 + lane]);
    fma8(acc, wq1, lds[L_A + 256 + 64 + lane], lds[L_A + 256 + 128 + 64 + lane]);
    float val = 0.f;
#pragma unroll
    for (int jj = 0; jj < 16; ++jj) { const float v = wave_sum(acc[jj]); val = (lane == jj) ? v : val; }
    if (lane < 16) stg((u64*)(ws + WS_GQP) + (qkg * 2 + (lane >> 3)) * 4096 + qcg * 64 + wave * 8 + (lane & 7), tag, __float_as_uint(val));
  }
  ST(3);
  drain(); ISSUE_LDS(0, pj + 16);
  { unsigned v[4];
    sweep<4>((const u64*)(ws + WS_GQP) + b * 4096 + kvh * 512 + tid, 8192, tag, v);
    const float q = (__uint_as_float(v[0]) + __uint_as_float(v[1])) + (__uint_as_float(v[2]) + __uint_as_float(v[3]));
    const float qn = dpp<XOR1>(q);
    if ((tid & 1) == 0) reinterpret_cast<unsigned*>(lds + L_Q)[tid >> 1] = cvtpk(q, qn); }
  drain(); ISSUE_LDS(1, pj + 24);
  bar();
  ST(4);
  u32x4 qpk[4];
#pragma unroll
  for (int g = 0; g < 4; ++g) qpk[g] = *reinterpret_cast<const u32x4*>(lds + L_Q + g * 64 + 4 * c);

  constexpr float C2 = SCALE * 1.4426950408889634f;
  float m = -INFINITY, l = 0.f, acc[32];
#pragma unroll
  for (int k = 0; k < 32; ++k) acc[k] = 0.f;
  float* prow = lds + L_P + (wave * 4 + r) * 16;
  auto proc = [&](const u32x4 (&Kc)[4], const u32x4 (&Vc)[4]) {
    float x[16];
#pragma unroll
    for (int t = 0; t < 4; ++t)
#pragma unroll
      for (int g = 0; g < 4; ++g) { float d = 0.f;
#pragma unroll
        for (int k = 0; k < 4; ++k) d = dot2(Kc[t][k], qpk[g][k], d);
        x[t * 4 + g] = d; }
#define TR_STEP(HALF, CTRL, BIT) _Pragma("unroll") for (int h = 0; h < (HALF); ++h) { \
      const float lo_ = x[h], hi_ = x[h + (HALF)]; const float t1_ = lo_ + dpp<CTRL>(lo_), t2_ = hi_ + dpp<CTRL>(hi_); \
      x[h] = (c & (BIT)) ? t2_ : t1_; }
    TR_STEP(8, XOR8, 8) TR_STEP(4, XOR7, 4) TR_STEP(2, XOR2, 2) TR_STEP(1, XOR1, 1)
#undef TR_STEP
    const float sc = x[0] * C2;
    float pm = sc; pm = fmaxf(pm, dpp<ROR4>(pm)); pm = fmaxf(pm, dpp<XOR8>(pm)); pm = xrow16_max(pm);
    const float mn = fmaxf(m, pm);
    const float alpha = __builtin_amdgcn_exp2f(m - mn), pp = __builtin_amdgcn_exp2f(sc - mn);
    l = fmaf(l, alpha, pp); m = mn;
#pragma unroll
    for (int q4 = 0; q4 < 4; ++q4) {
      const float al = q4 == 0 ? dpp<0x00>(alpha) : q4 == 1 ? dpp<0x55>(alpha) : q4 == 2 ? dpp<0xAA>(alpha) : dpp<0xFF>(alpha);
#pragma unroll
      for (int k = 0; k < 8; ++k) acc[q4 * 8 + k] *= al; }
    prow[c] = pp;
    asm volatile("s_waitcnt lgkmcnt(0)" ::: "memory");
    f32x4 pr[4];
#pragma unroll
    for (int u = 0; u < 4; ++u) pr[u] = *reinterpret_cast<const f32x4*>(prow + 4 * u);
#pragma unroll
    for (int t = 0; t < 4; ++t) {
      float vf[8];
#pragma unroll
      for (int k = 0; k < 4; ++k) { vf[2 * k] = lo2f(Vc[t][k]); vf[2 * k + 1] = hi2f(Vc[t][k]); }
#pragma unroll
      for (int g = 0; g < 4; ++g) { const float pg_ = pr[t][g];
#pragma unroll
        for (int k = 0; k < 8; ++k) acc[g * 8 + k] = fmaf(pg_, vf[k], acc[g * 8 + k]); } }
  };
  proc(K0, V0); __builtin_amdgcn_sched_barrier(0); ISSUE(K0, V0, pj + 4 * 8); __builtin_amdgcn_sched_barrier(0);
  proc(K1, V1); __builtin_amdgcn_sched_barrier(0);
  {
    asm volatile("s_waitcnt vmcnt(8)" ::: "memory");
    ISSUE(K1, V1, pj + 5 * 8);
#pragma unroll 1
    for (int i = 0; i < 2; ++i) {
      u32x4 Kt[4], Vt[4];
#pragma unroll
      for (int t = 0; t < 4; ++t) { Kt[t] = *reinterpret_cast<const u32x4*>(pgl + i * 2048 + t * 256 + lane * 4); Vt[t] = *reinterpret_cast<const u32x4*>(pgl + i * 2048 + 1024 + t * 256 + lane * 4); }
      proc(Kt, Vt);
    }
  }
  const u32x4 wm2 = *reinterpret_cast<const u32x4*>(p.wmod + (size_t)tid * 3072 + 1024 + sw * 8);
  const unsigned cpk2a = reinterpret_cast<const unsigned short*>(p.c)[tid], cpk2b = reinterpret_cast<const unsigned short*>(p.c)[512 + tid];
  { float acc[16];
#pragma unroll
    for (int jj = 0; jj < 16; ++jj) acc[jj] = 0.f;
    const float c0 = lo2f(cpk2a), c1 = lo2f(cpk2b);
    fma8(acc, wm2, rbf(c0 / (1.f + __expf(-c0))), rbf(c1 / (1.f + __expf(-c1))));
    reduce_tile<1>(acc, lds, tid);
    if (tid < 8) { const int bb = tid >> 2, pr = tid & 3;
      stg(gmod + bb * 1536 + 512 + sw * 4 + pr, tag, cvtpk(lds[L_RES + bb * 8 + 2 * pr], lds[L_RES + bb * 8 + 2 * pr + 1])); } }
  {
    asm volatile("s_waitcnt lgkmcnt(0)" ::: "memory");
    unsigned short* wt = reinterpret_cast<unsigned short*>(pgl);
    u32x4 t0 = *reinterpret_cast<const u32x4*>(p.wo + (size_t)(tkg * 256 + wave * 32 + (lane >> 2)) * 512 + tcg * 32 + (lane & 3) * 8);
    u32x4 t1 = *reinterpret_cast<const u32x4*>(p.wo + (size_t)(tkg * 256 + wave * 32 + 16 + (lane >> 2)) * 512 + tcg * 32 + (lane & 3) * 8);
    u32x4 t2 = u32x4{0, 0, 0, 0}, t3 = t2, t4 = t2;
    if (sw < 224) {
      t2 = *reinterpret_cast<const u32x4*>(p.wg + (size_t)(wave * 64 + lane) * FF + sw * 8);
      t3 = *reinterpret_cast<const u32x4*>(p.wu + (size_t)(wave * 64 + lane) * FF + sw * 8);
      t4 = *reinterpret_cast<const u32x4*>(p.wd + (size_t)(tkg * 128 + (wave >> 1) * 32 + (lane >> 1)) * 512 + tcg * 32 + (wave & 1) * 16 + (lane & 1) * 8);
    }
#pragma unroll
    for (int e = 0; e < 8; ++e) {
      const int sh = (e & 1) * 16, ix = e >> 1;
      const int n = (lane & 3) * 8 + e;
      wt[(n >> 4) * 512 + (n & 15) * 32 + (lane >> 2)] = (unsigned short)(t0[ix] >> sh);
      wt[(n >> 4) * 512 + (n & 15) * 32 + 16 + (lane >> 2)] = (unsigned short)(t1[ix] >> sh);
      wt[1024 + e * 64 + lane] = (unsigned short)(t2[ix] >> sh);
      wt[1024 + (8 + e) * 64 + lane] = (unsigned short)(t3[ix] >> sh);
      wt[2048 + ((lane & 1) * 8 + e) * 32 + (lane >> 1)] = (unsigned short)(t4[ix] >> sh);
    }
  }
#pragma unroll 1
  for (int i = 4; i < 16; i += 2) {
    proc(K0, V0); __builtin_amdgcn_sched_barrier(0);
    if (i + 2 < 16) ISSUE(K0, V0, pj + (i + 2) * 8);
    __builtin_amdgcn_sched_barrier(0);
    proc(K1, V1); __builtin_amdgcn_sched_barrier(0);
    if (i + 3 < 16) ISSUE(K1, V1, pj + (i + 3) * 8);
    __builtin_amdgcn_sched_barrier(0);
  }
#undef ISSUE
#undef ISSUE_LDS
  ST(5);
  { unsigned v[2], w[2];
    sweep<2>(gmod + 512 + tid, 512, tag, v); sweep<2>(gmod + 1536 + 512 + tid, 512, tag, w);
#pragma unroll
    for (int i = 0; i < 2; ++i) { const int pi = 512 + 512 * i + tid;
      reinterpret_cast<unsigned*>(lds)[L_MOD + pi] = v[i]; reinterpret_cast<unsigned*>(lds)[L_MOD + 1536 + pi] = w[i]; } }
#pragma unroll
  for (int k = 0; k < 32; ++k) acc[k] = xrow16_sum(acc[k]);
  { float t = l; t += dpp<ROR4>(t); t += dpp<XOR8>(t); l = xrow16_sum(t); }
  float* mrg = ldsP + 2048;
  if (r == 0) {
#pragma unroll
    for (int g = 0; g < 4; ++g) {
      float* rec = mrg + wave * 4096 + g * 132;
#pragma unroll
      for (int k = 0; k < 8; k += 4) *reinterpret_cast<f32x4*>(rec + 4 + 8 * c + k) = f32x4{acc[g * 8 + k], acc[g * 8 + k + 1], acc[g * 8 + k + 2], acc[g * 8 + k + 3]};
      if (c == g) { rec[0] = m; rec[1] = l; } } }
  drain();
  bar();
  {
    const int g = tid >> 7, d = tid & 127;
    float M = -INFINITY;
#pragma unroll
    for (int w = 0; w < NW; ++w) M = fmaxf(M, mrg[w * 4096 + g * 132]);
    const float Ms = (M == -INFINITY) ? 0.f : M;
    float L = 0.f, o = 0.f;
#pragma unroll
    for (int w = 0; w < NW; ++w) { const float* rec = mrg + w * 4096 + g * 132; const float wt = __builtin_amdgcn_exp2f(rec[0] - Ms);
      L = fmaf(wt, rec[1], L); o = fmaf(wt, rec[4 + d], o); }
    stg(po + (size_t)wg * 512 + tid, tag, __float_as_uint(o));
    if (d == 0) { stg(gml + (wg * 4 + g) * 2, tag, __float_as_uint(M)); stg(gml + (wg * 4 + g) * 2 + 1, tag, __float_as_uint(L)); }
  }
  ST(6);
  const unsigned short* wt = reinterpret_cast<const unsigned short*>(pgl);
  unsigned short* act = reinterpret_cast<unsigned short*>(lds + L_ACT);
  float* part = lds + L_RED;
  const int fn = lane & 15, fk = lane >> 4;
  {
    { const int bb = tid >> 8, hh = (tid >> 7) & 1, d = tid & 127, h = tkg * 2 + hh;
      const int src0 = (bb * 8 + (h >> 2)) * 16;
      unsigned v[16], ml[2];
      sweep<2>(gml + ((src0 + c) * 4 + (h & 3)) * 2, 1, tag, ml);
      prepoll(po + ((size_t)src0 * 4 + (h & 3)) * 128 + d + (size_t)(tid & 15) * 512, tag);
      sweep<16>(po + ((size_t)src0 * 4 + (h & 3)) * 128 + d, 512, tag, v);
      const float ms = __uint_as_float(ml[0]), ls = __uint_as_float(ml[1]);
      float M = ms; M = fmaxf(M, dpp<XOR1>(M)); M = fmaxf(M, dpp<XOR2>(M)); M = fmaxf(M, dpp<XOR7>(M)); M = fmaxf(M, dpp<XOR8>(M));
      const float w = __builtin_amdgcn_exp2f(ms - M);
      float L = (w > 0.f) ? w * ls : 0.f; L += dpp<XOR1>(L); L += dpp<XOR2>(L); L += dpp<XOR7>(L); L += dpp<XOR8>(L);
      const float wn = w / L;
      float o = 0.f;
#pragma unroll
      for (int ss = 0; ss < 16; ++ss) o = fmaf(__builtin_bit_cast(float, __builtin_amdgcn_readlane(__builtin_bit_cast(int, wn), ss)), __uint_as_float(v[ss]), o);
      act[bb * 512 + hh * 128 + d] = (unsigned short)cvtpk(o, o);
    }
    bar();
    ST(7);
    const s16x8 bf = *reinterpret_cast<const s16x8*>(act + (fn & 1) * 512 + wave * 32 + fk * 8);
    const s16x8 a0 = *reinterpret_cast<const s16x8*>(wt + fn * 32 + fk * 8), a1 = *reinterpret_cast<const s16x8*>(wt + 512 + fn * 32 + fk * 8);
    f32x4 d0 = {0.f, 0.f, 0.f, 0.f}, d1 = d0;
    d0 = __builtin_amdgcn_mfma_f32_16x16x32_bf16(a0, bf, d0, 0, 0, 0);
    d1 = __builtin_amdgcn_mfma_f32_16x16x32_bf16(a1, bf, d1, 0, 0, 0);
    if (fn < 2) { *reinterpret_cast<f32x4*>(part + wave * 64 + fn * 32 + fk * 4) = d0; *reinterpret_cast<f32x4*>(part + wave * 64 + fn * 32 + 16 + fk * 4) = d1; }
    bar();
    if (tid < 64) { float sum = 0.f;
#pragma unroll
      for (int w = 0; w < 8; ++w) sum += part[w * 64 + tid];
      stg(yp + (tkg * 2 + (tid >> 5)) * 512 + tcg * 32 + (tid & 31), tag, __float_as_uint(sum)); }
  }
  ST(8);
  if (wave == 0) {
    const int o = sw * 4 + (lane >> 4), k = lane & 15;
    const float xo = lo2f(reinterpret_cast<const unsigned short*>(p.x)[o]);
    unsigned v[1]; sweep<1>(yp + k * 1024 + o, 1, tag, v);
    float y = __uint_as_float(v[0]);
    y += dpp<XOR1>(y); y += dpp<XOR2>(y); y += dpp<XOR7>(y); y += dpp<XOR8>(y);
    const float x2 = rbf(xo + rbf(modv(lds, o >> 9, 1024 + (o & 511)) * rbf(y)));
    const float x2n = __shfl_xor(x2, 16);
    if ((lane & 31) == 0) stg((u64*)(ws + WS_GX2) + (o >> 1), tag, cvtpk(x2, x2n));
  }
  if (sw >= 224) return;
  {
    { unsigned v[1]; sweep<1>((const u64*)(ws + WS_GX2) + tid, 1, tag, v);
      lds[L_XS + 2 * tid] = lo2f(v[0]); lds[L_XS + 2 * tid + 1] = hi2f(v[0]); }
    bar();
    ST(9);
    ln_mod_all(lds, 1536, 2048, tid);
    const s16x8 b0 = *reinterpret_cast<const s16x8*>(act + (fn & 1) * 512 + wave * 64 + fk * 8), b1 = *reinterpret_cast<const s16x8*>(act + (fn & 1) * 512 + wave * 64 + 32 + fk * 8);
    const s16x8 a0 = *reinterpret_cast<const s16x8*>(wt + 1024 + fn * 64 + fk * 8), a1 = *reinterpret_cast<const s16x8*>(wt + 1024 + fn * 64 + 32 + fk * 8);
    f32x4 d0 = {0.f, 0.f, 0.f, 0.f};
    d0 = __builtin_amdgcn_mfma_f32_16x16x32_bf16(a0, b0, d0, 0, 0, 0);
    d0 = __builtin_amdgcn_mfma_f32_16x16x32_bf16(a1, b1, d0, 0, 0, 0);
    if (fn < 2) *reinterpret_cast<f32x4*>(part + wave * 64 + fn * 32 + fk * 4) = d0;
    bar();
    if (tid < 8) { const int bb = tid >> 2, pr = tid & 3; float g0 = 0.f, g1 = 0.f, u0 = 0.f, u1 = 0.f;
#pragma unroll
      for (int w = 0; w < 8; ++w) { const float* q = part + w * 64 + bb * 32; g0 += q[2 * pr]; g1 += q[2 * pr + 1]; u0 += q[8 + 2 * pr]; u1 += q[8 + 2 * pr + 1]; }
      stg(ghh + bb * 896 + sw * 4 + pr, tag, cvtpk(rbf(g0) * rbf(u0), rbf(g1) * rbf(u1))); }
  }
  ST(10);
  {
    if (tid < 128) { const int bb = tid >> 6, pi = tid & 63; unsigned v[1]; sweep<1>(ghh + bb * 896 + tkg * 64 + pi, 1, tag, v);
      reinterpret_cast<unsigned*>(act)[bb * 256 + pi] = v[0]; }
    bar();
    const s16x8 b0 = *reinterpret_cast<const s16x8*>(act + (fn & 1) * 512 + (wave >> 1) * 32 + fk * 8);
    const s16x8 a0 = *reinterpret_cast<const s16x8*>(wt + 2048 + fn * 32 + fk * 8);
    f32x4 d0 = {0.f, 0.f, 0.f, 0.f};
    d0 = __builtin_amdgcn_mfma_f32_16x16x32_bf16(a0, b0, d0, 0, 0, 0);
    if (fn < 2) *reinterpret_cast<f32x4*>(part + wave * 64 + fn * 32 + fk * 4) = d0;
    bar();
    if (tid < 64) { const int bb = tid >> 5, n = tid & 31, mt = n >> 4; float sum = 0.f;
#pragma unroll
      for (int ks = 0; ks < 4; ++ks) sum += part[(ks * 2 + mt) * 64 + bb * 32 + (n & 15)];
      stg(gop + (tkg * 2 + bb) * 512 + tcg * 32 + n, tag, __float_as_uint(sum)); }
  }
  ST(11);
  if (tkg == 0 && tid < 64) {
    const int bb = tid >> 5, cc = tid & 31, n = tcg * 32 + cc;
    unsigned v[14]; sweep<14>(gop + bb * 512 + n, 1024, tag, v);
    float y = 0.f;
#pragma unroll
    for (int k = 0; k < 14; ++k) y += __uint_as_float(v[k]);
    const float o = lds[L_XS + bb * 512 + n] + rbf(modv(lds, bb, 2560 + n) * rbf(y));
    p.out[bb * 512 + n] = (unsigned short)cvtpk(o, 0.f);
    if (sw == 0 && tid == 0) stw((unsigned*)(ws + WS_EPOCH), tag);
    ST(12);
  }
}

extern "C" void kernel_launch(void* const* d_in, const int* in_sizes, int n_in,
                              void* d_out, int out_size, void* d_ws, size_t ws_size,
                              hipStream_t stream) {
  Params p;
  p.x = (const bf16*)d_in[0]; p.kc = (const bf16*)d_in[1]; p.vc = (const bf16*)d_in[2];
  p.wq = (const bf16*)d_in[3]; p.wo = (const bf16*)d_in[4];
  p.wg = (const bf16*)d_in[6]; p.wu = (const bf16*)d_in[7]; p.wd = (const bf16*)d_in[8];
  p.c = (const bf16*)d_in[9]; p.wmod = (const bf16*)d_in[10];
  p.out = (unsigned short*)d_out; p.ws = (char*)d_ws;
  mega<<<NWG, NT, 0, stream>>>(p);
}
```
